```python
import torch

def _softmax(x, axis=-1):
    return torch.softmax(x, dim=axis)
def _gelu(x):
    return torch.nn.functional.gelu(x, approximate="tanh")
def _silu(x):
    return torch.nn.functional.silu(x)
def _sigmoid(x):
    return torch.sigmoid(x)
def _layernorm(x, axis=-1, eps=1e-5):
    m = x.mean(dim=axis, keepdim=True)
    v = x.var(dim=axis, keepdim=True, unbiased=False)
    return (x - m) / torch.sqrt(v + eps)
def _scatter_add(tgt, idx, src, axis=0):
    return tgt.clone().index_add_(axis, idx, src)
def _segment_sum(src, seg, n):
    out = torch.zeros((n,) + src.shape[1:], dtype=src.dtype)
    return out.index_add_(0, seg, src)
def _pad(x, pw):
    flat = tuple(v for pair in reversed(pw) for v in pair)
    return torch.nn.functional.pad(x, flat)

kernel_name = 'parallel_sum__spectral_gfnet__dense_activation_silu__prompt'

def setup_inputs():

    _w = (lambda a: (lambda t: t.to(torch.bfloat16) if t.is_floating_point() else t)(torch.as_tensor(a)))
    def _A_setup():
        B, S, D = 1, 2048, 512
        Df = D // 2 + 1
        return {
            "x": _w((lambda *s: torch.randn(*s, dtype=torch.bfloat16) * 0.05)(B, S, D)),
            "w_r": _w((lambda *s: torch.randn(*s, dtype=torch.bfloat16) * 0.8)(S, Df) * 0.02),
            "w_i": _w((lambda *s: torch.randn(*s, dtype=torch.bfloat16) * 0.8)(S, Df) * 0.02),
        }
    _A_in = _A_setup()

    def _B_setup():
        B, S, D, F = 1, 2048, 512, 1376
        return {
            "x": _w((lambda *s: torch.randn(*s, dtype=torch.bfloat16) * 0.05)(B, S, D)),
            "w1": _w((lambda *s: torch.randn(*s, dtype=torch.bfloat16) * 0.8)(D, F) * (D ** -0.5)),
            "w2": _w((lambda *s: torch.randn(*s, dtype=torch.bfloat16) * 0.1)(F, D) * (F ** -0.5)),
        }
    _B_in = _B_setup()

    return {
        "A_x": _A_in["x"],
        "A_w_r": _A_in["w_r"],
        "A_w_i": _A_in["w_i"],
        "B_x": _B_in["x"],
        "B_w1": _B_in["w1"],
        "B_w2": _B_in["w2"],
    }

def reference(A_x, A_w_r, A_w_i, B_x, B_w1, B_w2):
    def _A_ref(x, w_r, w_i):
        B, S, D = x.shape
        X = (lambda x, axes=(-2,-1): torch.fft.rfft2(x.float(), dim=axes))(x, axes=(-2, -1))
        W = w_r + 1j * w_i
        X = X * W
        y = (lambda x, s, axes=(-2,-1): torch.fft.irfft2(x, s=s, dim=axes).to(torch.bfloat16))(X, (S, D), axes=(-2, -1))
        return y

    def _B_ref(x, w1, w2):
        h = torch.einsum("bsd,df->bsf", x, w1)
        h = _silu(h)
        out = torch.einsum("bsf,fd->bsd", h, w2)
        return out

    x = A_x
    xn = _layernorm(x, axis=-1)
    return x + _A_ref(xn, A_w_r, A_w_i) + _B_ref(xn, B_w1, B_w2)
```

```cpp
#include <hip/hip_runtime.h>
#include <hip/hip_bf16.h>

typedef unsigned u32;
typedef unsigned short u16;
typedef unsigned long long u64;
typedef u32 u32x4 __attribute__((ext_vector_type(4)));
typedef u32 u32x2 __attribute__((ext_vector_type(2)));
typedef float f32x4 __attribute__((ext_vector_type(4)));
typedef float f32x2 __attribute__((ext_vector_type(2)));
typedef __bf16 bf16x8 __attribute__((ext_vector_type(8)));
typedef __bf16 bf16x2 __attribute__((ext_vector_type(2)));
typedef _Float16 f16x8 __attribute__((ext_vector_type(8)));
typedef _Float16 f16x2 __attribute__((ext_vector_type(2)));
typedef short s16x4 __attribute__((ext_vector_type(4)));
#define LAS __attribute__((address_space(3)))
#define GAS __attribute__((address_space(1)))
#define DEVI __device__ __forceinline__

constexpr int S_ = 2048, D_ = 512, F_ = 1376, DF = 257;
constexpr u32 WS_CTRL = 0;
constexpr u32 WS_T = 65536;
constexpr u32 WS_H = WS_T + (2u << 20);
constexpr u32 WS_A = WS_H + 32u * 4 * 43 * 1024;
constexpr u32 WS_END = WS_A + (2u << 20);
constexpr int XROW = 1040;
constexpr int LDS_X = 0;
constexpr int W1CH = 11264;
constexpr int NSLOT = 8;
constexpr int LDS_W1 = 64 * XROW;
constexpr int LDS_ST = LDS_W1 + NSLOT * W1CH;
constexpr int LDS_ST2 = LDS_ST + 2048;
constexpr int LDS_BYTES = 163840;

DEVI float asf(u32 v) { return __builtin_bit_cast(float, v); }
DEVI u32 asu(float v) { return __builtin_bit_cast(u32, v); }
DEVI float cosrev(float x) { return __builtin_amdgcn_cosf(x); }
DEVI u32 pk_bf16(float a, float b) { f32x2 v = {a, b}; return __builtin_bit_cast(u32, __builtin_convertvector(v, bf16x2)); }
DEVI u32 pk_f16(float a, float b) { f32x2 v = {a, b}; return __builtin_bit_cast(u32, __builtin_convertvector(v, f16x2)); }
DEVI float rbf(float a) { return __builtin_bit_cast(float, pk_bf16(a, a) << 16); }
DEVI float bf2f(u32 h) { return __builtin_bit_cast(float, h << 16); }
DEVI f32x4 mfma_bf(u32x4 a, u32x4 b, f32x4 c) {
  return __builtin_amdgcn_mfma_f32_16x16x32_bf16(__builtin_bit_cast(bf16x8, a), __builtin_bit_cast(bf16x8, b), c, 0, 0, 0);
}
DEVI f32x4 mfma_h(u32x4 a, u32x4 b, f32x4 c) {
  return __builtin_amdgcn_mfma_f32_16x16x32_f16(__builtin_bit_cast(f16x8, a), __builtin_bit_cast(f16x8, b), c, 0, 0, 0);
}
DEVI float dot2bf(u32 a, u32 b, float c) {
  return __builtin_amdgcn_fdot2_f32_bf16(__builtin_bit_cast(bf16x2, a), __builtin_bit_cast(bf16x2, b), c, false);
}
DEVI u32x4 mk_srd(const void* p, u32 bytes) {
  u64 a = (u64)p;
  u32x4 r = {(u32)a, (u32)(a >> 32) & 0xffffu, bytes, 0x00020000u};
  return r;
}
DEVI void dma16(u32x4 srd, u32 voff, u32 soff, u32 lds) {
  asm volatile("s_mov_b32 m0, %3\n\ts_nop 0\n\tbuffer_load_dwordx4 %0, %1, %2 offen lds"
               :: "v"(voff), "s"(srd), "s"(soff), "s"(lds) : "memory");
}
#define WAITV(n) asm volatile("s_waitcnt vmcnt(" #n ")" ::: "memory")
#define BAR() asm volatile("s_waitcnt lgkmcnt(0)\n\ts_barrier" ::: "memory")
DEVI u32x4 lds128(LAS unsigned char* L, int off) { return *(LAS u32x4*)(L + off); }
DEVI u32x2 ldstr(LAS unsigned char* L, int off) {
  return __builtin_bit_cast(u32x2, __builtin_amdgcn_ds_read_tr16_b64_v4i16((LAS s16x4*)(L + off)));
}
DEVI float ror8(float v) {
  return __builtin_bit_cast(float, __builtin_amdgcn_update_dpp(0, __builtin_bit_cast(int, v), 0x128, 0xf, 0xf, false));
}
constexpr int AUX = 0;

DEVI void block_ij(int& i, int& j) {
  const int b = blockIdx.x, xc = b & 7, idx = b >> 3;
  i = 8 * (xc >> 1) + (idx >> 2);
  j = 4 * (xc & 1) + (idx & 3);
}
DEVI float gm_val(int rho, int s1) {
  int k1 = rho >> 1; float ph = (rho & 1) ? -0.25f : 0.f;
  if (rho == 1) { k1 = 32; ph = 0.f; }
  return cosrev((float)((s1 * k1) & 63) * (1.f / 64.f) + ph);
}
DEVI float m_val(int p, int phi, int pq, int s2, int which) {
  int k; float ph; float msk = 1.f;
  if (p != 0) { k = p + 64 * phi; ph = 0.25f * (float)(which - pq); }
  else {
    k = phi < 16 ? 64 * phi : 64 * phi - 992; ph = -0.25f * (float)pq;
    msk = (which == (phi >> 4)) ? 1.f : 0.f;
    if (phi == 0 && pq == 1) { k = 1024; ph = 0.f; }
  }
  return msk * cosrev((float)((s2 * k) & 2047) * (1.f / 2048.f) + ph);
}

__global__ __launch_bounds__(256, 1) void k_p1(const u16* __restrict__ x, const u16* __restrict__ w1, unsigned char* __restrict__ ws) {
  extern __shared__ __attribute__((aligned(16))) unsigned char smem[];
  LAS unsigned char* L = (LAS unsigned char*)smem;
  const int tid = threadIdx.x, lane = tid & 63, w = __builtin_amdgcn_readfirstlane(tid >> 6), g = lane >> 4, c = lane & 15;
  int i, j; block_ij(i, j);
  const u32 lbase = (u32)(size_t)L;
  const u32x4 sx = mk_srd(x, S_ * D_ * 2), sw1 = mk_srd(w1, D_ * F_ * 2);
  const __amdgpu_buffer_rsrc_t rws = __builtin_amdgcn_make_buffer_rsrc((void*)ws, 0, (int)WS_END, 0x00020000);
#pragma unroll
  for (int n = 0; n < 16; ++n) {
    const int r = 16 * (n >> 2) + 4 * w + (n & 3);
    dma16(sx, lane * 16, (32 * r + i) * 1024, lbase + LDS_X + r * XROW);
  }
  const int u0 = j < 6 ? 11 * j : 66 + 10 * (j - 6), nt = j < 6 ? 11 : 10;
  u32 w1off[3];
#pragma unroll
  for (int n = 0; n < 3; ++n) {
    const int e = n * 64 + lane, rr = e / 22; int ch = e - rr * 22;
    if (ch >= 2 * nt) ch -= 2;
    const int rp = 8 * w + (rr & 7), h = rp >> 4, gg = (rp >> 2) & 3, q4 = rp & 3;
    w1off[n] = (128 * gg + 4 * h + q4) * (F_ * 2) + u0 * 32 + ch * 16;
  }
  auto w1_issue = [&](int ks) {
    const u32 dst = lbase + LDS_W1 + (ks % NSLOT) * W1CH + w * 2816;
    dma16(sw1, w1off[0], ks * 8 * F_ * 2, dst);
    dma16(sw1, w1off[1], ks * 8 * F_ * 2, dst + 1024);
    if (lane < 48) dma16(sw1, w1off[2], ks * 8 * F_ * 2, dst + 2048);
  };
#pragma unroll
  for (int ks = 0; ks < NSLOT; ++ks) w1_issue(ks);
  u32x4 Ff[16];
  {
    int kd = 32 * j + 8 * w + (c & 7); float ph = (c >> 3) ? 0.25f : 0.f;
    if (kd == 0 && (c >> 3)) { kd = 256; ph = 0.f; }
    const float step = (float)kd * (1.f / 512.f);
#pragma unroll
    for (int n = 0; n < 16; ++n) {
      const int ks = (n + 4 * w) & 15, k0 = 128 * g + 8 * ks;
      const float a0 = (float)((k0 * kd) & 511) * (1.f / 512.f) + ph;
#pragma unroll
      for (int q = 0; q < 4; ++q) Ff[n][q] = pk_bf16(cosrev(a0 + (2 * q) * step), cosrev(a0 + (2 * q + 1) * step));
    }
  }
  u32x4 Gf[4][2];
#pragma unroll
  for (int t = 0; t < 4; ++t)
#pragma unroll
    for (int kp = 0; kp < 2; ++kp)
#pragma unroll
      for (int q = 0; q < 4; ++q) {
        const int s1 = 32 * kp + 16 * (q >> 1) + 4 * g + 2 * (q & 1);
        Gf[t][kp][q] = pk_f16(gm_val(16 * t + c, s1), gm_val(16 * t + c, s1 + 1));
      }
  f32x4 xa[4]; float ssum[4], ssq[4];
  int abase[4];
#pragma unroll
  for (int a = 0; a < 4; ++a) abase[a] = LDS_X + c * XROW + (16 * g + 4 * ((a + w) & 3)) * 16;
#pragma unroll
  for (int t = 0; t < 4; ++t) {
    if (t == 0) WAITV(36); else if (t == 1) WAITV(32); else if (t == 2) WAITV(28); else WAITV(24);
    BAR();
    xa[t] = f32x4{0.f, 0.f, 0.f, 0.f}; ssum[t] = 0.f; ssq[t] = 0.f;
#pragma unroll
    for (int n = 0; n < 16; ++n) {
      const u32x4 a = lds128(L, abase[n >> 2] + (n & 3) * 16 + t * 16 * XROW);
      xa[t] = mfma_bf(a, Ff[n], xa[t]);
      if (n < 4) {
#pragma unroll
        for (int q = 0; q < 4; ++q) { ssum[t] = dot2bf(a[q], 0x3f803f80u, ssum[t]); ssq[t] = dot2bf(a[q], a[q], ssq[t]); }
      }
    }
  }
  {
    float s2v[2], q2v[2];
#pragma unroll
    for (int a = 0; a < 2; ++a) {
      auto r1 = __builtin_amdgcn_permlane32_swap(__builtin_bit_cast(u32, ssum[a]), __builtin_bit_cast(u32, ssum[a + 2]), false, false);
      s2v[a] = asf(r1[0]) + asf(r1[1]);
      auto r2 = __builtin_amdgcn_permlane32_swap(__builtin_bit_cast(u32, ssq[a]), __builtin_bit_cast(u32, ssq[a + 2]), false, false);
      q2v[a] = asf(r2[0]) + asf(r2[1]);
    }
    auto r1 = __builtin_amdgcn_permlane16_swap(__builtin_bit_cast(u32, s2v[0]), __builtin_bit_cast(u32, s2v[1]), false, false);
    auto r2 = __builtin_amdgcn_permlane16_swap(__builtin_bit_cast(u32, q2v[0]), __builtin_bit_cast(u32, q2v[1]), false, false);
    f32x2 o = {asf(r1[0]) + asf(r1[1]),
               asf(r2[0]) + asf(r2[1])};
    *(LAS f32x2*)(L + LDS_ST + (w * 64 + lane) * 8) = o;
  }
  WAITV(0);
  BAR();
  {
    float sm = 0.f, sq = 0.f;
#pragma unroll
    for (int a = 0; a < 4; ++a) { f32x2 v = *(LAS f32x2*)(L + LDS_ST + (a * 64 + lane) * 8); sm += v[0]; sq += v[1]; }
    const float mean = sm * (1.f / 512.f);
    const float var = fmaxf(sq * (1.f / 512.f) - mean * mean, 0.f);
    const float sd = rbf(__builtin_amdgcn_sqrtf(rbf(rbf(var) + 1e-5f)));
    *(LAS float*)(L + LDS_ST2 + w * 512 + lane * 4) = rbf(mean);
    *(LAS float*)(L + LDS_ST2 + w * 512 + 256 + lane * 4) = __builtin_amdgcn_rcpf(sd);
  }
  {
    u32x4 xb[2];
    const float mco = (j == 0 && w == 0 && c == 0) ? 512.f : 0.f;
#pragma unroll
    for (int t = 0; t < 4; ++t) {
      const f32x4 mv = *(LAS f32x4*)(L + LDS_ST2 + w * 512 + (16 * t + 4 * g) * 4);
      const f32x4 rv = *(LAS f32x4*)(L + LDS_ST2 + w * 512 + 256 + (16 * t + 4 * g) * 4);
      float v[4];
#pragma unroll
      for (int r = 0; r < 4; ++r) v[r] = (xa[t][r] - mco * mv[r]) * (rv[r] * 0.03125f);
      xb[t >> 1][2 * (t & 1)] = pk_f16(v[0], v[1]);
      xb[t >> 1][2 * (t & 1) + 1] = pk_f16(v[2], v[3]);
    }
#pragma unroll
    for (int t = 0; t < 4; ++t) {
      f32x4 tt = mfma_h(Gf[t][0], xb[0], f32x4{0.f, 0.f, 0.f, 0.f});
      tt = mfma_h(Gf[t][1], xb[1], tt);
#pragma unroll
      for (int sub = 0; sub < 2; ++sub) {
        const int p = 8 * t + 2 * g + sub;
        __builtin_amdgcn_raw_buffer_store_b32(pk_f16(tt[2 * sub], tt[2 * sub + 1]), rws,
            WS_T + ((((j * 32 + p) * 4 + w) * 32 + i) * 16 + c) * 4, 0, AUX);
      }
    }
  }
  f32x4 acc[3][4], cs[3];
#pragma unroll
  for (int u = 0; u < 3; ++u) { cs[u] = f32x4{0.f, 0.f, 0.f, 0.f};
#pragma unroll
    for (int t = 0; t < 4; ++t) acc[u][t] = f32x4{0.f, 0.f, 0.f, 0.f}; }
  const int xbase = LDS_X + c * XROW + 16 * g * 16;
  const int wbase = LDS_W1 + (4 * g + (c >> 2)) * 352 + (3 * w) * 32 + (c & 3) * 8;
  const u32x4 ones = {0x3f803f80u, 0x3f803f80u, 0x3f803f80u, 0x3f803f80u};
  u32x4 xf[2][4], wf[2][3];
  auto rd = [&](int ks, int b) {
#pragma unroll
    for (int u = 0; u < 3; ++u) {
      const int uo = (w == 3 && u == 2) ? 32 : u * 32;
      const u32x2 lo = ldstr(L, wbase + (ks % NSLOT) * W1CH + uo), hi = ldstr(L, wbase + (ks % NSLOT) * W1CH + uo + 16 * 352);
      wf[b][u] = u32x4{lo[0], lo[1], hi[0], hi[1]};
    }
#pragma unroll
    for (int t = 0; t < 4; ++t) xf[b][t] = lds128(L, xbase + ks * 16 + t * 16 * XROW);
  };
  rd(0, 0);
#pragma unroll
  for (int ks = 0; ks < 16; ++ks) {
    if (ks + 1 >= NSLOT && ks + 1 < 16) {
      const int last = (ks - 1 + NSLOT) < 15 ? (ks - 1 + NSLOT) : 15;
      const int n = 3 * (last - (ks + 1));
      if (n == 18) WAITV(18); else if (n == 15) WAITV(15); else if (n == 12) WAITV(12); else if (n == 9) WAITV(9);
      else if (n == 6) WAITV(6); else if (n == 3) WAITV(3); else WAITV(0);
    }
    BAR();
    if (ks + NSLOT < 16) w1_issue(ks + NSLOT);
    if (ks + 1 < 16) rd(ks + 1, (ks + 1) & 1);
#pragma unroll
    for (int u = 0; u < 3; ++u) {
      cs[u] = mfma_bf(wf[ks & 1][u], ones, cs[u]);
#pragma unroll
      for (int t = 0; t < 4; ++t) acc[u][t] = mfma_bf(wf[ks & 1][u], xf[ks & 1][t], acc[u][t]);
    }
  }
  float mrow[4], rrow[4];
#pragma unroll
  for (int t = 0; t < 4; ++t) {
    mrow[t] = *(LAS float*)(L + LDS_ST2 + w * 512 + (16 * t + c) * 4);
    rrow[t] = *(LAS float*)(L + LDS_ST2 + w * 512 + 256 + (16 * t + c) * 4);
  }
#pragma unroll
  for (int u = 0; u < 3; ++u) {
    const int ul = 3 * w + u;
    if (ul < nt) {
      const int ug = u0 + ul;
      u32 hp[4][2];
#pragma unroll
      for (int t = 0; t < 4; ++t) {
        float v[4];
#pragma unroll
        for (int r = 0; r < 4; ++r) {
          const float hv = (acc[u][t][r] - mrow[t] * cs[u][r]) * rrow[t];
          v[r] = hv * __builtin_amdgcn_rcpf(1.f + __builtin_amdgcn_exp2f(hv * -1.4426950408889634f));
        }
        hp[t][0] = pk_bf16(v[0], v[1]); hp[t][1] = pk_bf16(v[2], v[3]);
      }
#pragma unroll
      for (int tp = 0; tp < 2; ++tp) {
        auto a = __builtin_amdgcn_permlane16_swap(hp[2 * tp][0], hp[2 * tp + 1][0], false, false);
        auto b = __builtin_amdgcn_permlane16_swap(hp[2 * tp][1], hp[2 * tp + 1][1], false, false);
        const u32x4 o = {a[0], b[0], a[1], b[1]};
        const int t = 2 * tp + (g & 1), gp = 2 * (ug & 1) + (g >> 1);
        __builtin_amdgcn_raw_buffer_store_b128(o, rws, WS_H + ((i * 4 + t) * 43 + (ug >> 1)) * 1024 + (gp * 16 + c) * 16, 0, AUX);
      }
    }
  }
}

__global__ __launch_bounds__(256, 1) void k_p2(const u16* __restrict__ wr, const u16* __restrict__ wi, unsigned char* __restrict__ ws) {
  const int tid = threadIdx.x, lane = tid & 63, w = __builtin_amdgcn_readfirstlane(tid >> 6), g = lane >> 4, c = lane & 15;
  int p, j; block_ij(p, j);
  const __amdgpu_buffer_rsrc_t rws = __builtin_amdgcn_make_buffer_rsrc((void*)ws, 0, (int)WS_END, 0x00020000);
  u32x4 tb[2];
#pragma unroll
  for (int n = 0; n < 8; ++n)
    tb[n >> 2][n & 3] = __builtin_amdgcn_raw_buffer_load_b32(rws, WS_T + ((j * 32 + p) * 4 + w) * 2048 + n * 256 + lane * 4, 0, AUX);
  float wa[8], wb[8], wc[8], wd[8], waO, wbO;
  {
    const int kd = 32 * j + 8 * w + (c & 7);
    const bool spec = kd == 0;
    const int col = (spec && (c >> 3)) ? 256 : kd;
    const float nrm = spec ? (1.f / 32768.f) : (2.f / 32768.f);
    const float sg = spec ? 0.f : ((c >> 3) ? nrm : -nrm);
#pragma unroll
    for (int e = 0; e < 8; ++e) {
      const int phi = 8 * (e >> 1) + 2 * g + (e & 1);
      int kp = p != 0 ? p + 64 * phi : (phi < 16 ? 64 * phi : 64 * phi - 992);
      int km = (2048 - kp) & 2047;
      const bool z = (e == 0) && kp == 0;
      if (z) km = 1024;
      const float rp = bf2f(wr[kp * DF + col]), ip = bf2f(wi[kp * DF + col]);
      const float rm = bf2f(wr[km * DF + col]), im = bf2f(wi[km * DF + col]);
      wa[e] = (rp + rm) * nrm; wb[e] = (ip + im) * sg;
      wc[e] = (im - ip) * nrm; wd[e] = (rp - rm) * sg;
      if (e == 0) {
        waO = wa[0]; wbO = wb[0];
        if (z) { wa[0] = rp * nrm; wb[0] = ip * sg; waO = rm * nrm; wbO = im * sg; wc[0] = 0.f; wd[0] = 0.f; }
      }
    }
  }
  u32x4 eo[2];
#pragma unroll
  for (int t = 0; t < 4; ++t) {
    f32x4 pq = f32x4{0.f, 0.f, 0.f, 0.f};
#pragma unroll
    for (int kp = 0; kp < 2; ++kp) {
      u32x4 mf;
#pragma unroll
      for (int q = 0; q < 4; ++q) {
        const int s2 = 4 * (4 * kp + q) + g, phi = 8 * t + (c >> 1);
        mf[q] = pk_f16(m_val(p, phi, c & 1, s2, 0), m_val(p, phi, c & 1, s2, 1));
      }
      pq = mfma_h(mf, tb[kp], pq);
    }
#pragma unroll
    for (int sub = 0; sub < 2; ++sub) {
      const int e = 2 * t + sub;
      const float P = pq[2 * sub], Q = pq[2 * sub + 1], Po = ror8(P), Qo = ror8(Q);
      const float E = P * wa[e] + Po * wb[e] - Q * wc[e] - Qo * wd[e];
      const float O = P * wc[e] + Po * wd[e] + Q * (e == 0 ? waO : wa[e]) + Qo * (e == 0 ? wbO : wb[e]);
      eo[t >> 1][2 * (t & 1) + sub] = pk_bf16(E, O);
    }
  }
  u32 vp[4][2];
#pragma unroll
  for (int nu = 0; nu < 4; ++nu) {
    f32x4 v = f32x4{0.f, 0.f, 0.f, 0.f};
#pragma unroll
    for (int kp = 0; kp < 2; ++kp) {
      u32x4 mi;
#pragma unroll
      for (int q = 0; q < 4; ++q) {
        const int phi = 16 * kp + 8 * (q >> 1) + 2 * g + (q & 1), s2 = 8 * nu + (c >> 1);
        mi[q] = pk_bf16(m_val(p, phi, 0, s2, c & 1), m_val(p, phi, 1, s2, c & 1));
      }
      v = mfma_bf(eo[kp], mi, v);
    }
    vp[nu][0] = pk_bf16(v[0], v[1]); vp[nu][1] = pk_bf16(v[2], v[3]);
  }
#pragma unroll
  for (int np = 0; np < 2; ++np) {
    auto a = __builtin_amdgcn_permlane16_swap(vp[2 * np][0], vp[2 * np + 1][0], false, false);
    auto b = __builtin_amdgcn_permlane16_swap(vp[2 * np][1], vp[2 * np + 1][1], false, false);
    const u32x4 o = {a[0], b[0], a[1], b[1]};
    const int nu = 2 * np + (g & 1), s2 = 8 * nu + (c >> 1), reim = g >> 1;
    const int ks = 2 * j + (w >> 1), gp = 2 * (w & 1) + reim, m = 2 * (p & 7) + (c & 1);
    __builtin_amdgcn_raw_buffer_store_b128(o, rws, WS_A + ((s2 * 4 + (p >> 3)) * 16 + ks) * 1024 + (gp * 16 + m) * 16, 0, AUX);
  }
}

constexpr int LDS_W2 = 0;
constexpr int W2SL = 22;
constexpr int LDS_FI = W2SL * 4096;
constexpr int LDS_PM = 0;
constexpr int PMROW = 144;
static_assert(LDS_FI + 65536 <= LDS_BYTES, "lds");
__global__ __launch_bounds__(256, 1) void k_p3(const u16* __restrict__ x, const u16* __restrict__ w2, unsigned char* __restrict__ ws, u16* __restrict__ out) {
  extern __shared__ __attribute__((aligned(16))) unsigned char smem[];
  LAS unsigned char* L = (LAS unsigned char*)smem;
  const int tid = threadIdx.x, lane = tid & 63, w = __builtin_amdgcn_readfirstlane(tid >> 6), g = lane >> 4, c = lane & 15;
  int i, j; block_ij(i, j);
  const u32 lbase = (u32)(size_t)L;
  const u32x4 sw2 = mk_srd(w2, D_ * F_ * 2);
  const __amdgpu_buffer_rsrc_t rws = __builtin_amdgcn_make_buffer_rsrc((void*)ws, 0, (int)WS_END, 0x00020000);
  u32 w2off;
  {
    const int row = 8 * w + (lane >> 3), pos = lane & 7, swz = ((row >> 1) & 1) | (((row >> 3) & 1) << 1);
    w2off = row * (D_ * 2) + j * 128 + ((((pos >> 1) ^ swz) << 1) | (pos & 1)) * 16;
  }
  auto w2_issue = [&](int ks) { dma16(sw2, w2off, ks * 32 * D_ * 2, lbase + LDS_W2 + (ks % W2SL) * 4096 + w * 1024); };
#pragma unroll
  for (int ks = 0; ks < W2SL; ++ks) w2_issue(ks);
  u32x4 hf[43];
#pragma unroll
  for (int ks = 0; ks < 22; ++ks) hf[ks] = __builtin_amdgcn_raw_buffer_load_b128(rws, WS_H + ((i * 4 + w) * 43 + ks) * 1024 + lane * 16, 0, AUX);
  {
    const int d = 64 * j + 16 * w + c;
    const float step = (float)d * (1.f / 512.f), ph = (g & 1) ? 0.25f : 0.f;
#pragma unroll
    for (int ks = 0; ks < 16; ++ks) {
      const int kd0 = 16 * ks + 8 * (g >> 1);
      const float a0 = (float)((kd0 * d) & 511) * (1.f / 512.f) + ph;
      u32x4 f;
#pragma unroll
      for (int q = 0; q < 4; ++q) {
        float v0 = cosrev(a0 + (2 * q) * step);
        if (ks == 0 && q == 0 && g == 1) v0 = (d & 1) ? -1.f : 1.f;
        f[q] = pk_bf16(v0, cosrev(a0 + (2 * q + 1) * step));
      }
      *(LAS u32x4*)(L + LDS_FI + (ks * 4 + w) * 1024 + lane * 16) = f;
    }
  }
  u32x4 gi[2];
#pragma unroll
  for (int kp = 0; kp < 2; ++kp)
#pragma unroll
    for (int q = 0; q < 4; ++q) gi[kp][q] = pk_bf16(gm_val(32 * kp + 8 * g + 2 * q, 16 * w + c), gm_val(32 * kp + 8 * g + 2 * q + 1, 16 * w + c));
  f32x4 acc[4];
#pragma unroll
  for (int nu = 0; nu < 4; ++nu) acc[nu] = f32x4{0.f, 0.f, 0.f, 0.f};
  int wb[4];
  {
    const int row = 8 * g + (c >> 2), swz = ((row >> 1) & 1) | (((row >> 3) & 1) << 1);
#pragma unroll
    for (int nu = 0; nu < 4; ++nu) wb[nu] = LDS_W2 + row * 128 + ((nu ^ swz) * 32) + (c & 3) * 8;
  }
  WAITV(0); BAR();
#pragma unroll
  for (int ks = 0; ks < 22; ++ks) {
#pragma unroll
    for (int nu = 0; nu < 4; ++nu) {
      const u32x2 lo = ldstr(L, wb[nu] + (ks % W2SL) * 4096), hi = ldstr(L, wb[nu] + (ks % W2SL) * 4096 + 512);
      acc[nu] = mfma_bf(u32x4{lo[0], lo[1], hi[0], hi[1]}, hf[ks], acc[nu]);
    }
  }
  BAR();
#pragma unroll
  for (int ks = 22; ks < 43; ++ks) w2_issue(ks);
#pragma unroll
  for (int ks = 22; ks < 43; ++ks) hf[ks] = __builtin_amdgcn_raw_buffer_load_b128(rws, WS_H + ((i * 4 + w) * 43 + ks) * 1024 + lane * 16, 0, AUX);
  u32x4 af[16];
#pragma unroll
  for (int ks = 0; ks < 16; ++ks) af[ks] = __builtin_amdgcn_raw_buffer_load_b128(rws, WS_A + ((i * 4 + w) * 16 + ks) * 1024 + lane * 16, 0, AUX);
  f32x4 pm[4];
#pragma unroll
  for (int nu = 0; nu < 4; ++nu) pm[nu] = f32x4{0.f, 0.f, 0.f, 0.f};
  WAITV(0); BAR();
#pragma unroll
  for (int ks = 22; ks < 43; ++ks) {
#pragma unroll
    for (int nu = 0; nu < 4; ++nu) {
      const u32x2 lo = ldstr(L, wb[nu] + (ks % W2SL) * 4096), hi = ldstr(L, wb[nu] + (ks % W2SL) * 4096 + 512);
      acc[nu] = mfma_bf(u32x4{lo[0], lo[1], hi[0], hi[1]}, hf[ks], acc[nu]);
    }
  }
#pragma unroll
  for (int ks = 0; ks < 16; ++ks)
#pragma unroll
    for (int nu = 0; nu < 4; ++nu) pm[nu] = mfma_bf(af[ks], lds128(L, LDS_FI + (ks * 4 + nu) * 1024 + lane * 16), pm[nu]);
  BAR();
#pragma unroll
  for (int nu = 0; nu < 4; ++nu) {
    const u32x2 o = {pk_bf16(pm[nu][0], pm[nu][1]), pk_bf16(pm[nu][2], pm[nu][3])};
    *(LAS u32x2*)(L + LDS_PM + (16 * nu + c) * PMROW + (16 * w + 4 * g) * 2) = o;
  }
  BAR();
#pragma unroll
  for (int nu = 0; nu < 4; ++nu)
#pragma unroll
    for (int kp = 0; kp < 2; ++kp) acc[nu] = mfma_bf(lds128(L, LDS_PM + (16 * nu + c) * PMROW + (32 * kp + 8 * g) * 2), gi[kp], acc[nu]);
  const int row = 32 * (16 * w + c) + i;
#pragma unroll
  for (int nu = 0; nu < 4; ++nu) {
    const u32x2 xv = *(const GAS u32x2*)(x + row * D_ + 64 * j + 16 * nu + 4 * g);
    const u32x2 o = {pk_bf16(acc[nu][0] + bf2f(xv[0] & 0xffffu), acc[nu][1] + __builtin_bit_cast(float, xv[0] & 0xffff0000u)),
                     pk_bf16(acc[nu][2] + bf2f(xv[1] & 0xffffu), acc[nu][3] + __builtin_bit_cast(float, xv[1] & 0xffff0000u))};
    *(GAS u32x2*)(out + row * D_ + 64 * j + 16 * nu + 4 * g) = o;
  }
}

extern "C" void kernel_launch(void* const* d_in, const int* in_sizes, int n_in, void* d_out, int out_size, void* d_ws, size_t ws_size,
                              hipStream_t stream) {
  const u16* x = (const u16*)d_in[0]; const u16* wr = (const u16*)d_in[1]; const u16* wi = (const u16*)d_in[2];
  const u16* w1 = (const u16*)d_in[4]; const u16* w2 = (const u16*)d_in[5];
  (void)hipFuncSetAttribute((const void*)k_p1, hipFuncAttributeMaxDynamicSharedMemorySize, LDS_BYTES);
  (void)hipFuncSetAttribute((const void*)k_p3, hipFuncAttributeMaxDynamicSharedMemorySize, LDS_BYTES);
  k_p1<<<256, 256, LDS_BYTES, stream>>>(x, w1, (unsigned char*)d_ws);
  k_p2<<<256, 256, 0, stream>>>(wr, wi, (unsigned char*)d_ws);
  k_p3<<<256, 256, LDS_BYTES, stream>>>(x, w2, (unsigned char*)d_ws, (u16*)d_out);
}
```

```cpp
#include <hip/hip_runtime.h>
#include <hip/hip_bf16.h>

typedef unsigned u32;
typedef unsigned short u16;
typedef unsigned long long u64;
typedef u32 u32x4 __attribute__((ext_vector_type(4)));
typedef u32 u32x2 __attribute__((ext_vector_type(2)));
typedef float f32x4 __attribute__((ext_vector_type(4)));
typedef float f32x2 __attribute__((ext_vector_type(2)));
typedef __bf16 bf16x8 __attribute__((ext_vector_type(8)));
typedef __bf16 bf16x2 __attribute__((ext_vector_type(2)));
typedef _Float16 f16x8 __attribute__((ext_vector_type(8)));
typedef _Float16 f16x2 __attribute__((ext_vector_type(2)));
typedef short s16x4 __attribute__((ext_vector_type(4)));
#define LAS __attribute__((address_space(3)))
#define GAS __attribute__((address_space(1)))
#define DEVI __device__ __forceinline__

constexpr int S_ = 2048, D_ = 512, F_ = 1376, DF = 257;
constexpr u32 WS_EPOCH = 0;
constexpr u32 WS_FV = 65536;
constexpr u32 WS_FT = 131072;
constexpr u32 WS_FH = 262144;
constexpr u32 WS_T = 393216;
constexpr u32 WS_H = WS_T + (2u << 20);
constexpr u32 WS_A = WS_H + 32u * 4 * 43 * 1024;
constexpr u32 WS_END = WS_A + (2u << 20);
constexpr int XROW = 1040;
constexpr int LDS_X = 0;
constexpr int W1CH = 11264;
constexpr int NSLOT = 7;
constexpr int LDS_RING = 64 * XROW;
constexpr int LDS_TT = LDS_RING + NSLOT * W1CH;
constexpr int LDS_ST = LDS_TT + 8192;
constexpr int LDS_ST2 = LDS_ST + 2048;
constexpr int LDS_PROG = LDS_ST2 + 2048;
constexpr int LDS_TAG = LDS_PROG + 2048;
constexpr int LDS_DUMMY = LDS_TAG + 512;
constexpr int LDS_BYTES = 163840;
constexpr int LDS_FI = 0;
constexpr int U2N = 9;
constexpr int LDS_PM = LDS_TT, PMROW = 144;
constexpr int LDS_RED = 0;
constexpr int LDS_PROG2 = LDS_DUMMY + 1024;
DEVI constexpr int u2slot(int n) { return LDS_RING + (n % U2N) * 8192; }
constexpr int PC_STAT = 1, PC_T = 2, PC_W1 = 3  , PC_H = 19, PC_FI = 20, PC_W2 = 22  , PC_RED = 50;
constexpr int PL_W1 = 5, PL_W2 = 21;
constexpr int SPIN_CAP = 1 << 15;
constexpr u32 SENT = 0xffffffffu;

DEVI float asf(u32 v) { return __builtin_bit_cast(float, v); }
DEVI u32 asu(float v) { return __builtin_bit_cast(u32, v); }
DEVI float cosrev(float x) { return __builtin_amdgcn_cosf(x); }
DEVI u32 pk_bf16(float a, float b) { f32x2 v = {a, b}; return __builtin_bit_cast(u32, __builtin_convertvector(v, bf16x2)); }
DEVI u32 pk_f16(float a, float b) { f32x2 v = {a, b}; return __builtin_bit_cast(u32, __builtin_convertvector(v, f16x2)); }
DEVI u32 pk_bf16v(f32x2 v) { return __builtin_bit_cast(u32, __builtin_convertvector(v, bf16x2)); }
DEVI u32 pk_f16v(f32x2 v) { return __builtin_bit_cast(u32, __builtin_convertvector(v, f16x2)); }
DEVI float rbf(float a) { return asf(pk_bf16(a, a) << 16); }
DEVI float bf2f(u32 h) { return asf(h << 16); }
DEVI f32x4 mfma_bf(u32x4 a, u32x4 b, f32x4 c) {
  return __builtin_amdgcn_mfma_f32_16x16x32_bf16(__builtin_bit_cast(bf16x8, a), __builtin_bit_cast(bf16x8, b), c, 0, 0, 0);
}
DEVI f32x4 mfma_h(u32x4 a, u32x4 b, f32x4 c) {
  return __builtin_amdgcn_mfma_f32_16x16x32_f16(__builtin_bit_cast(f16x8, a), __builtin_bit_cast(f16x8, b), c, 0, 0, 0);
}
DEVI float dot2bf(u32 a, u32 b, float c) {
  return __builtin_amdgcn_fdot2_f32_bf16(__builtin_bit_cast(bf16x2, a), __builtin_bit_cast(bf16x2, b), c, false);
}
DEVI u32x4 mk_srd(const void* p, u32 bytes) {
  u64 a = (u64)p;
  u32x4 r = {(u32)a, (u32)(a >> 32) & 0xffffu, bytes, 0x00020000u};
  return r;
}
DEVI void dma16(u32x4 srd, u32 voff, u32 soff, u32 lds) {
  asm volatile("s_mov_b32 m0, %3\n\ts_nop 0\n\tbuffer_load_dwordx4 %0, %1, %2 offen lds"
               :: "v"(voff), "s"(srd), "s"(soff), "s"(lds) : "memory");
}
DEVI void dma_x8(u32x4 srd, u32 voff, u32 soff, u32 lds) {
  u32 t;
#define DX_ "s_add_u32 m0, m0, 0x410\n\ts_add_u32 %0, %0, 0x8000\n\tbuffer_load_dwordx4 %1, %2, %0 offen lds\n\t"
  asm volatile("s_mov_b32 m0, %4\n\ts_mov_b32 %0, %3\n\tbuffer_load_dwordx4 %1, %2, %0 offen lds\n\t" DX_ DX_ DX_ DX_ DX_ DX_ DX_
               : "=&s"(t) : "v"(voff), "s"(srd), "s"(soff), "s"(lds) : "memory", "scc");
#undef DX_
}
DEVI void dma_4k(u32x4 srd, u32 v0, u32 v1, u32 v2, u32 v3, u32 soff, u32 lds) {
  asm volatile("s_mov_b32 m0, %6\n\ts_nop 0\n\tbuffer_load_dwordx4 %0, %4, %5 offen lds\n\tbuffer_load_dwordx4 %1, %4, %5 offen offset:1024 lds\n\t"
               "buffer_load_dwordx4 %2, %4, %5 offen offset:2048 lds\n\tbuffer_load_dwordx4 %3, %4, %5 offen offset:3072 lds"
               :: "v"(v0), "v"(v1), "v"(v2), "v"(v3), "s"(srd), "s"(soff), "s"(lds) : "memory");
}
DEVI void dma_w1tail(u32x4 srd, u32 v4, u32 v5, u32 soff) {
  asm volatile("s_add_u32 m0, m0, 0x1000\n\ts_nop 0\n\tbuffer_load_dwordx4 %0, %2, %3 offen lds\n\ts_mov_b32 exec_hi, 0\n\t"
               "buffer_load_dwordx4 %1, %2, %3 offen offset:1024 lds\n\ts_mov_b32 exec_hi, -1"
               :: "v"(v4), "v"(v5), "s"(srd), "s"(soff) : "memory", "scc");
}
#define WAITV(n) asm volatile("s_waitcnt vmcnt(" #n ")" ::: "memory")
#define WAITL() asm volatile("s_waitcnt lgkmcnt(0)" ::: "memory")
#define FENCE() asm volatile("" ::: "memory")
#define SCHED() __builtin_amdgcn_sched_barrier(0)
#define BAR0() do { asm volatile("s_waitcnt lgkmcnt(0)\n\ts_barrier" ::: "memory"); SCHED(); } while (0)
#define NOUNROLL _Pragma("unroll 1")
DEVI u32x4 lds128(LAS unsigned char* L, int off) { return *(LAS u32x4*)(L + off); }
DEVI u32x2 ldstr(LAS unsigned char* L, int off) {
  return __builtin_bit_cast(u32x2, __builtin_amdgcn_ds_read_tr16_b64_v4i16((LAS s16x4*)(L + off)));
}
DEVI float ror8(float v) { return asf(__builtin_amdgcn_mov_dpp((int)asu(v), 0x128, 0xf, 0xf, true)); }
constexpr int SC1 = 16;
constexpr u32 OOB = 0x7ffffff0u;
DEVI void st128(u32x4 v, __amdgpu_buffer_rsrc_t rs, u32 off) { __builtin_amdgcn_raw_buffer_store_b128(v, rs, off, 0, SC1); }
DEVI void st_flag(u32x2 tag, __amdgpu_buffer_rsrc_t rs, u32 off, int lane) { __builtin_amdgcn_raw_buffer_store_b64(tag, rs, lane == 0 ? off : OOB, 0, SC1); }
#define MK_RWS() __builtin_amdgcn_make_buffer_rsrc((void*)ws, 0, (int)WS_END, 0x00020000)
template <class T> DEVI void pin(T& v) { asm volatile("" : "+v"(v)); }
#define SGB(mask, n) __builtin_amdgcn_sched_group_barrier(mask, n, 0)
DEVI u32 umax(u32 a, u32 b) { return a > b ? a : b; }
DEVI u32 max4(u32x4 v) { return umax(umax(v[0], v[1]), umax(v[2], v[3])); }
DEVI bool any_sent(u32 m) { return __builtin_amdgcn_ballot_w64(m == SENT) != 0ull; }

#define STAMP(n)
#define STAMP_DECL
#define STAMP_OUT()

#define STAMPX(n) STAMP(n)
DEVI void prog_set(LAS unsigned char* L, int w, int lane, u32 v) {
  FENCE(); *(volatile LAS u32*)(L + LDS_PROG + w * 256 + lane * 4) = v; FENCE();
}
DEVI int pga_of(int first, int cnt, int lane) { int a = LDS_PROG + (first + (lane & (cnt - 1))) * 260; pin(a); return a; }
DEVI u32 prog_get(LAS unsigned char* L, int pga) { return *(volatile LAS u32*)(L + pga); }
DEVI bool prog_bad(u32 v, u32 target) { return __builtin_expect(__builtin_amdgcn_ballot_w64(v < target) != 0ull, 0); }
template <int SL = 0> DEVI void prog_spin(LAS unsigned char* L, int pga, u32 target) {
  NOUNROLL
  for (int s = 0; s < SPIN_CAP * 8; ++s) { if (SL) __builtin_amdgcn_s_sleep(SL); if (__builtin_amdgcn_ballot_w64(prog_get(L, pga) < target) == 0ull) break; }
  FENCE();
}
template <int SL = 0> DEVI void prog_wait(LAS unsigned char* L, int pga, u32 target) {
  if (prog_bad(prog_get(L, pga), target)) prog_spin<SL>(L, pga, target);
  FENCE();
}
template <int NL, int SL> DEVI void flag_wait(__amdgpu_buffer_rsrc_t rs, u32 off, int lane, int nlanes, u32x2 tag) {
  bool miss[NL];
#pragma unroll
  for (int n = 0; n < NL; ++n) miss[n] = lane < nlanes;
  NOUNROLL
  for (int s = 0; s < SPIN_CAP; ++s) {
    FENCE();
    u32x2 f[NL];
#pragma unroll
    for (int n = 0; n < NL; ++n) f[n] = __builtin_amdgcn_raw_buffer_load_b64(rs, miss[n] ? off + (n * 64 + lane) * 128 : 0x7ffffff0u, 0, 16);
    bool any = false;
#pragma unroll
    for (int n = 0; n < NL; ++n) { miss[n] = miss[n] && !(f[n][0] == tag[0] && f[n][1] == tag[1]); any |= miss[n]; }
    if (__builtin_amdgcn_ballot_w64(any) == 0ull) break;
    __builtin_amdgcn_s_sleep(SL);
  }
  FENCE();
}

DEVI void gen32(float a0, float step, float K, u32x4* o) {
  f32x2 p = {cosrev(a0), cosrev(a0 + step)}, q = {cosrev(a0 + 2.f * step), cosrev(a0 + 3.f * step)};
  o[0][0] = pk_bf16v(p); o[0][1] = pk_bf16v(q);
#pragma unroll
  for (int m = 2; m < 16; ++m) { const f32x2 r = K * q - p; p = q; q = r; o[m >> 2][m & 3] = pk_bf16v(r); }
}

#define TFSTEP 5
#define X_NWR 4
#define X_HAUX SC1
#define TSLEEP 8

__global__ __launch_bounds__(512, 2) void k_all(const u16* __restrict__ x, const u16* __restrict__ wr, const u16* __restrict__ wi,
                                                 const u16* __restrict__ w1, const u16* __restrict__ w2, unsigned char* __restrict__ ws,
                                                 u16* __restrict__ out) {
  extern __shared__ __attribute__((aligned(16))) unsigned char smem[];
  LAS unsigned char* L = (LAS unsigned char*)smem;
  const int tid = threadIdx.x, lane = tid & 63, w = __builtin_amdgcn_readfirstlane(tid >> 6), g = lane >> 4, c = lane & 15;
  STAMP_DECL STAMP(0);
  prog_set(L, w, lane, 0);
  const int xc = blockIdx.x & 7, idx = blockIdx.x >> 3;
  const int i = 8 * (xc >> 1) + (idx >> 2), j = 4 * (xc & 1) + (idx & 3);
  const int i3 = 4 * xc + (idx >> 3), j3 = idx & 7;
  const int rotx = idx & 3, rot1 = 2 * (idx >> 2), rot2 = (5 * (idx & 7) + 11 * (idx >> 3)) % 43;
  const int u0 = 11 * j - (j == 7), nt = 11 - (j >= 6);

  if (w >= 4 && w < 6) {
    __builtin_amdgcn_s_setprio(3);
    const int l = w - 4;
    const u32 lbase = (u32)(size_t)L;
    const u32x4 sx = mk_srd(x, S_ * D_ * 2), sw1 = mk_srd((const char*)w1 - 4096, D_ * F_ * 2 + 4096), sw2 = mk_srd(w2, D_ * F_ * 2);
#define X_ISSUE(t) dma_x8(sx, lane * 16, (32 * ((16 * (t) + 8 * l + 16 * rotx) & 63) + i) * 1024, lbase + LDS_X + (16 * (t) + 8 * l) * XROW)
    X_ISSUE(0);
    BAR0();
    X_ISSUE(1);
    SCHED();
    u32 w1off[6];
#pragma unroll
    for (int n = 0; n < 6; ++n) {
      const int e = n * 64 + lane, rr = (e * 745) >> 14; int ch = e - rr * 22;
      if (ch >= 2 * nt) ch -= 2;
      w1off[n] = (rr & 3) * (F_ * 2) + ((rr >> 2) & 3) * (128 * F_ * 2) + ch * 16 + (4 * l * (F_ * 2) + u0 * 32 + 4096 - (n & 3) * 1024);
      pin(w1off[n]);
    }
    SCHED();
    X_ISSUE(2); WAITV(16); prog_set(L, w, lane, 1);
    X_ISSUE(3); WAITV(16); prog_set(L, w, lane, 2);
    WAITV(8); prog_set(L, w, lane, 3);
    STAMP(1);
    const int gC = pga_of(0, 4, lane);
    {
      u32 kk = rot1, pv = 0;
      const u32 d0 = lbase + LDS_RING + l * 5632;
      u32 w2off[4];
#pragma unroll
      for (int n = 0; n < 16; ++n) {
        if (n >= NSLOT) { if (prog_bad(pv, PC_W1 + (n - NSLOT))) prog_spin<1>(L, gC, PC_W1 + (n - NSLOT)); }
        const u32 so = kk * (8 * F_ * 2);
        dma_4k(sw1, w1off[0], w1off[1], w1off[2], w1off[3], so, d0 + ((n + 5) % NSLOT) * W1CH);
        kk = (kk + 1) & 15;
        dma_w1tail(sw1, w1off[4], w1off[5], so);
        if (n + 1 >= NSLOT && n + 1 < 16) pv = prog_get(L, gC);
        if (n == 0) { WAITV(6); prog_set(L, w, lane, 4); }
        else if (n >= 2 && n != NSLOT && n != NSLOT + 1) { WAITV(12); prog_set(L, w, lane, 3 + n); }
        if (n == NSLOT - 1) {
#pragma unroll
          for (int q = 0; q < 4; ++q) {
            const int row = 8 * q + (lane >> 3), pos = lane & 7, swz = ((row >> 1) & 1) | (((row >> 3) & 1) << 1);
            w2off[q] = row * (D_ * 2) + 128 * j3 + ((((pos >> 1) ^ swz) << 1) | (pos & 1)) * 16 - q * 1024;
            pin(w2off[q]);
          }
          WAITV(6); prog_set(L, w, lane, 4 + n); WAITV(0); prog_set(L, w, lane, 5 + n);
        }
      }
      WAITV(6); prog_set(L, w, lane, PL_W1 + 14);
      WAITV(0); prog_set(L, w, lane, PL_W1 + 15);
      STAMP(2);
      const __amdgpu_buffer_rsrc_t rw2 = __builtin_amdgcn_make_buffer_rsrc((void*)w2, 0, D_ * F_ * 2, 0x00020000);
      u32x4 stg[52];
      u32 so = (rot2 + l + 2 * U2N) * (32 * D_ * 2); if (so >= 43 * 32 * D_ * 2) so -= 43 * 32 * D_ * 2;
#pragma unroll
      for (int n = 0; n < 13; ++n) {
#pragma unroll
        for (int q = 0; q < 4; ++q) stg[n * 4 + q] = __builtin_amdgcn_raw_buffer_load_b128(rw2, w2off[q] + q * 1024, so, 0);
        so += 2 * 32 * D_ * 2; if (so >= 43 * 32 * D_ * 2) so -= 43 * 32 * D_ * 2;
        FENCE();
      }
      so = (rot2 + l) * (32 * D_ * 2); if (so >= 43 * 32 * D_ * 2) so -= 43 * 32 * D_ * 2;
      const u32 d2 = lbase + l * 4096;
      prog_wait<1>(L, gC, PC_W1 + 15);
      STAMP(4);
#pragma unroll
      for (int n = 0; n < U2N; ++n) {
        dma_4k(sw2, w2off[0], w2off[1], w2off[2], w2off[3], so, d2 + u2slot(n));
        so += 2 * 32 * D_ * 2; if (so >= 43 * 32 * D_ * 2) so -= 43 * 32 * D_ * 2;
      }
      WAITV(0); prog_set(L, w, lane, PL_W2 + U2N - 1);
      STAMP(5);
      int wa0 = LDS_RING + l * 4096 + lane * 16, wa1 = wa0 + 4 * 8192; pin(wa0); pin(wa1);
#pragma unroll
      for (int n = U2N; n < 22; ++n) {
        prog_wait<1>(L, gC, PC_W2 + n - U2N);
#pragma unroll
        for (int q = 0; q < X_NWR; ++q) *(LAS u32x4*)(L + ((n % U2N) < 4 ? wa0 + (n % U2N) * 8192 : wa1 + ((n % U2N) - 4) * 8192) + q * 1024) = stg[(n - U2N) * 4 + q];
        prog_set(L, w, lane, PL_W2 + n);
      }
    }
    STAMP(3);
    STAMP_OUT();
    return;
  }

  if (w >= 6) {
    const int q = w - 6;
    const __amdgpu_buffer_rsrc_t rws = MK_RWS();
    const int p = (i & ~1) + q, hq = i & 1;
    const int gC = pga_of(0, 4, lane), gP = pga_of(6, 2, lane), gP0 = pga_of(6, 1, lane);
    u32x2 ep = {0u, 0u};
    if (q == 0) ep = __builtin_amdgcn_raw_buffer_load_b64(rws, WS_EPOCH, 0, SC1);
    BAR0();
    u32x2 tag;
    {
      u32x4 mf[4][2], mi[4][2];
      const int pq = c & 1;
#pragma unroll
      for (int t = 0; t < 4; ++t) {
        const int phi = 8 * t + (c >> 1);
        int k = p + 64 * phi; float ph0 = -0.25f * (float)pq, ph1 = ph0 + 0.25f, m0 = 1.f, m1 = 1.f;
        if (p == 0) {
          ph1 = ph0;
          if (t < 2) m1 = 0.f; else { m0 = 0.f; k -= 992; }
          if (t == 0 && c == 1) { k = 1024; ph0 = 0.f; }
        }
        const float kf = (float)k * (1.f / 2048.f), a = (float)g * kf, b = a + 4.f * kf, K = 2.f * cosrev(4.f * kf);
        f32x2 v0 = {cosrev(a + ph0) * m0, cosrev(a + ph1) * m1}, v1 = {cosrev(b + ph0) * m0, cosrev(b + ph1) * m1};
        mf[t][0][0] = pk_f16v(v0); mf[t][0][1] = pk_f16v(v1);
#pragma unroll
        for (int n = 2; n < 8; ++n) { const f32x2 v2 = K * v1 - v0; v0 = v1; v1 = v2; mf[t][n >> 2][n & 3] = pk_f16v(v2); }
      }
#pragma unroll
      for (int t = 0; t < 4; ++t) { pin(mf[t][0]); pin(mf[t][1]); }
      SCHED();
      if (q == 0) {
        const u64 t = (((u64)ep[1] << 32) | ep[0]) + 1;
        *(LAS u32x2*)(L + LDS_TAG + lane * 8) = u32x2{(u32)t, (u32)(t >> 32)};
        prog_set(L, w, lane, 1);
      }
      SCHED();
      prog_wait<2>(L, pga_of(4, 2, lane), 4);
      u32 raw[8][4];
      {
        const __amdgpu_buffer_rsrc_t rwr = __builtin_amdgcn_make_buffer_rsrc((void*)wr, 0, S_ * DF * 2, 0x00020000);
        const __amdgpu_buffer_rsrc_t rwi = __builtin_amdgcn_make_buffer_rsrc((void*)wi, 0, S_ * DF * 2, 0x00020000);
        const int colb = (j == 0 && hq == 0 && c == 8) ? 510 : (32 * j + 16 * hq + 2 * (c & 7)) * 2;
        const int vp = 128 * g * (DF * 2) + colb, vm = (384 - 128 * g) * (DF * 2) + colb;
        const int vm0 = ((p == 0 && g == 0) ? 1024 : 2048 - p - 128 * g) * (DF * 2) + colb;
#pragma unroll
        for (int e = 0; e < 8; ++e) {
          const int ku = p + 512 * (e >> 1) + 64 * (e & 1) - ((e >= 4 && p == 0) ? 992 : 0);
          raw[e][0] = __builtin_amdgcn_raw_buffer_load_b32(rwr, vp, ku * (DF * 2), 0);
          raw[e][1] = __builtin_amdgcn_raw_buffer_load_b32(rwi, vp, ku * (DF * 2), 0);
          raw[e][2] = __builtin_amdgcn_raw_buffer_load_b32(rwr, e == 0 ? vm0 : vm, e == 0 ? 0 : (1664 - ku) * (DF * 2), 0);
          raw[e][3] = __builtin_amdgcn_raw_buffer_load_b32(rwi, e == 0 ? vm0 : vm, e == 0 ? 0 : (1664 - ku) * (DF * 2), 0);
        }
      }
      SCHED();
      {
        const int which = c & 1;
        const u32 msk0 = (p == 0 && which) ? 0u : ~0u, msk1 = (p == 0 && !which) ? 0u : ~0u;
        const u32 sgn = (p != 0 && (c & 2)) ? 0x80008000u : 0u;
#pragma unroll
        for (int nu = 0; nu < 4; ++nu) {
          const float s2 = (float)(8 * nu + (c >> 1));
          const float beta = p != 0 ? s2 * (float)p * (1.f / 2048.f) + 0.25f * (float)which : (which ? s2 * (1.f / 64.f) : 0.f);
#pragma unroll
          for (int r = 0; r < 4; ++r) {
            const float a = beta + s2 * (float)(8 * (r >> 1) + 2 * g + (r & 1)) * (1.f / 32.f);
            float hi = cosrev(a - 0.25f);
            if (r == 0 && p == 0 && g == 0 && !which) hi = (c & 2) ? -1.f : 1.f;
            const u32 v = pk_bf16(cosrev(a), hi);
            mi[nu][0][r] = v & msk0; mi[nu][1][r] = (v ^ sgn) & msk1;
          }
        }
      }
#pragma unroll
      for (int t = 0; t < 4; ++t) { pin(mf[t][0]); pin(mf[t][1]); pin(mi[t][0]); pin(mi[t][1]); }
      SCHED();
      STAMP(1);
      f32x2 cA[2][8], cB[2][8]; float aO[2], bO[2];
      if (j == 0 && hq == 0) {
#pragma unroll
        for (int e = 0; e < 8; ++e)
#pragma unroll
          for (int r = 0; r < 4; ++r) {
            const u32 o = asu(ror8(asf(raw[e][r])));
            raw[e][r] = c == 8 ? (raw[e][r] >> 16) | (o & 0xffff0000u) : raw[e][r];
          }
      }
#pragma unroll
      for (int tl = 0; tl < 2; ++tl) {
        const bool spec = tl == 0 && j == 0 && hq == 0 && (c & 7) == 0;
        const float nrm = spec ? (1.f / 32768.f) : (2.f / 32768.f);
        const float sg = spec ? 0.f : ((c >> 3) ? nrm : -nrm);
#pragma unroll
        for (int e = 0; e < 8; ++e) {
          const float rp = tl ? asf(raw[e][0] & 0xffff0000u) : asf(raw[e][0] << 16), ip = tl ? asf(raw[e][1] & 0xffff0000u) : asf(raw[e][1] << 16);
          const float rm = tl ? asf(raw[e][2] & 0xffff0000u) : asf(raw[e][2] << 16), im = tl ? asf(raw[e][3] & 0xffff0000u) : asf(raw[e][3] << 16);
          cA[tl][e] = (f32x2{rp, im} + f32x2{rm, -ip}) * nrm; cB[tl][e] = (f32x2{ip, rp} + f32x2{im, -rm}) * sg;
          if (e == 0) {
            aO[tl] = cA[tl][0][0]; bO[tl] = cB[tl][0][0];
            if (p == 0 && g == 0) { cA[tl][0] = f32x2{rp * nrm, 0.f}; cB[tl][0] = f32x2{ip * sg, 0.f}; aO[tl] = rm * nrm; bO[tl] = im * sg; }
          }
        }
      }
#pragma unroll
      for (int e = 0; e < 16; ++e) { pin(cA[e >> 3][e & 7]); pin(cB[e >> 3][e & 7]); }
      pin(aO[0]); pin(aO[1]); pin(bO[0]); pin(bO[1]);
      const int aoff = WS_A + (((16 * q + (lane >> 4)) * 4 + (p >> 3)) * 16 + ((lane >> 3) & 1) * 8 + j) * 1024 +
                       ((2 * hq + ((lane >> 2) & 1)) * 16 + 2 * (p & 6) + (lane & 3)) * 16;
      prog_wait<1>(L, gP0, 1);
      tag = *(LAS u32x2*)(L + LDS_TAG + lane * 8);
      STAMP(2);
      __builtin_amdgcn_s_setprio(2);
      if (q == 0) {
        prog_wait<2>(L, gC, PC_W1 + TFSTEP + 1);
        prog_wait<2>(L, pga_of(4, 2, lane), PL_W1 + 15);
        STAMP(3);
        flag_wait<1, 4>(rws, WS_FT + (j * 4 + 2 * hq) * 32 * 128, lane, 64, tag);
        prog_set(L, w, lane, 2);
      } else prog_wait<1>(L, gP0, 2);
      STAMP(5);
      u32 tb[2][8];
#pragma unroll
      for (int n = 0; n < 8; ++n)
#pragma unroll
        for (int tl = 0; tl < 2; ++tl)
          tb[tl][n] = __builtin_amdgcn_raw_buffer_load_b32(rws, WS_T + ((j * 32 + p) * 32 + g) * 256 + hq * 128 + tl * 64 + c * 4, n * 1024, SC1);
      SCHED();
      f32x4 pqv[2][4];
#pragma unroll
      for (int tl = 0; tl < 2; ++tl) {
        const u32x4 b0 = {tb[tl][0], tb[tl][1], tb[tl][2], tb[tl][3]}, b1 = {tb[tl][4], tb[tl][5], tb[tl][6], tb[tl][7]};
#pragma unroll
        for (int t = 0; t < 4; ++t) { pqv[tl][t] = mfma_h(mf[t][0], b0, f32x4{0.f, 0.f, 0.f, 0.f}); pqv[tl][t] = mfma_h(mf[t][1], b1, pqv[tl][t]); }
      }
      STAMP(6);
      u32x4 eo[2][2];
#pragma unroll
      for (int tl = 0; tl < 2; ++tl) {
        f32x2 r[8]; float Po[8], Qo[8];
#pragma unroll
        for (int e = 0; e < 8; ++e) { Po[e] = ror8(pqv[tl][e >> 1][2 * (e & 1)]); r[e] = pqv[tl][e >> 1][2 * (e & 1)] * cA[tl][e]; }
        SCHED();
#pragma unroll
        for (int e = 0; e < 8; ++e) { Qo[e] = ror8(pqv[tl][e >> 1][2 * (e & 1) + 1]); r[e] += Po[e] * cB[tl][e]; }
        SCHED();
#pragma unroll
        for (int e = 0; e < 8; ++e) r[e] += pqv[tl][e >> 1][2 * (e & 1) + 1] * f32x2{-cA[tl][e][1], e == 0 ? aO[tl] : cA[tl][e][0]};
        SCHED();
#pragma unroll
        for (int e = 0; e < 8; ++e) r[e] += Qo[e] * f32x2{-cB[tl][e][1], e == 0 ? bO[tl] : cB[tl][e][0]};
        SCHED();
#pragma unroll
        for (int e = 0; e < 8; ++e) eo[tl][e >> 2][e & 3] = pk_bf16v(r[e]);
        SCHED();
#pragma unroll
        for (int nu = 0; nu < 4; ++nu) pqv[tl][nu] = mfma_bf(eo[tl][0], mi[nu][0], f32x4{0.f, 0.f, 0.f, 0.f});
#pragma unroll
        for (int nu = 0; nu < 4; ++nu) pqv[tl][nu] = mfma_bf(eo[tl][1], mi[nu][1], pqv[tl][nu]);
        SCHED();
      }
      {
        const int sa = LDS_TT + ((((c >> 1) * 2 + (g >> 1)) * 2) * 4 + 2 * q + (c & 1)) * 16 + (g & 1) * 8;
#pragma unroll
        for (int tl = 0; tl < 2; ++tl)
#pragma unroll
          for (int nu = 0; nu < 4; ++nu)
            *(LAS u32x2*)(L + sa + nu * 2048 + tl * 64) = u32x2{pk_bf16(pqv[tl][nu][0], pqv[tl][nu][1]), pk_bf16(pqv[tl][nu][2], pqv[tl][nu][3])};
      }
      prog_set(L, w, lane, 3);
      prog_wait(L, gP, 3);
#pragma unroll
      for (int n = 0; n < 4; ++n) {
        const u32x4 v = lds128(L, LDS_TT + ((q * 4 + n) * 64 + lane) * 16);
        st128(v, rws, aoff + n * 262144);
      }
      STAMP(7);
      WAITV(0);
      st_flag(tag, rws, WS_FV + (q * 256 + blockIdx.x) * 128, lane);
    }
    STAMP(8);
    prog_set(L, w, lane, 4);
    __builtin_amdgcn_s_sleep(TSLEEP);
    STAMP(9);
    if (q == 0) {
      flag_wait<4, 8>(rws, WS_FV + (i3 >> 4) * 256 * 128, lane, 64, tag);
      prog_set(L, w, lane, 5);
      if (blockIdx.x == 0 && lane == 0) __builtin_amdgcn_raw_buffer_store_b64(tag, rws, WS_EPOCH, 0, SC1);
    } else {
      prog_wait<2>(L, gC, PC_H);
      STAMP(10);
      __builtin_amdgcn_s_sleep(TSLEEP);
      flag_wait<1, 4>(rws, WS_FH + i3 * 32 * 128, lane, 32, tag);
      prog_set(L, w, lane, 5);
    }
    STAMP(11);
    STAMP_OUT();
    return;
  }

  *(volatile LAS u32*)(L + LDS_PROG2 + w * 256 + lane * 4) = 0u;
  u32x4 Ff[16];
  {
    int kd = 32 * j + 16 * (w >> 1) + 2 * (c & 7) + (w & 1); float ph = (c >> 3) ? 0.25f : 0.f;
    if (kd == 0 && (c >> 3)) { kd = 256; ph = -0.25f; }
    const float step = (float)kd * (1.f / 512.f), K = 2.f * cosrev(2.f * step);
#pragma unroll
    for (int a = 0; a < 4; ++a) {
      const int k0 = 128 * g + 32 * ((a + w) & 3);
      gen32((float)(((2 * k0 + 1) * kd) & 1023) * (1.f / 1024.f) + ph, step, K, &Ff[4 * a]);
      if (a == 0) { pin(Ff[0]); pin(Ff[1]); pin(Ff[2]); pin(Ff[3]); BAR0(); }
    }
  }
  u32x4 Gf[4][2];
  {
    const float ph = (c & 1) ? -0.25f : 0.f;
    const u32 sgn = (c & 2) ? 0x80008000u : 0u;
#pragma unroll
    for (int t = 0; t < 4; ++t) {
      const float kf = (float)(8 * t + (c >> 1)) * (1.f / 64.f);
#pragma unroll
      for (int r = 0; r < 4; ++r) {
        const float a = (float)(16 * (r >> 1) + 4 * g + 2 * (r & 1) + 16 * rotx) * kf + ph;
        u32 v = pk_f16(cosrev(a), cosrev(a + kf));
        if (t == 0 && c == 1) v = 0xbc003c00u;
        Gf[t][0][r] = v; Gf[t][1][r] = v ^ sgn;
      }
    }
  }
#pragma unroll
  for (int n = 0; n < 16; ++n) pin(Ff[n]);
#pragma unroll
  for (int t = 0; t < 4; ++t) { pin(Gf[t][0]); pin(Gf[t][1]); }
  STAMP(1);
  const f32x4 z4 = {0.f, 0.f, 0.f, 0.f};
  const int gC = pga_of(0, 4, lane), gL = pga_of(4, 2, lane);
#define gP pga_of(6, 2, lane)
#define gP0 pga_of(6, 1, lane)
#define gP1 pga_of(7, 1, lane)
  f32x4 xa[4];
  {
    f32x4 sacc = z4, qacc[4];
    int abase[4];
#pragma unroll
    for (int a = 0; a < 4; ++a) abase[a] = LDS_X + c * XROW + (16 * g + 4 * ((a + w) & 3)) * 16;
    u32x4 af[16];
    prog_wait(L, gL, 1);
    STAMP(2);
#pragma unroll
    for (int n = 0; n < 16; ++n) af[n] = lds128(L, abase[n >> 2] + (n & 3) * 16);
    u32 pv = prog_get(L, gL);
#pragma unroll
    for (int t = 0; t < 4; ++t) {
      if (t < 3) { if (prog_bad(pv, t + 2)) prog_spin(L, gL, t + 2); }
      const u32 sv = c == t ? 0x3f803f80u : 0u;
      const u32x4 sel = {sv, sv, sv, sv};
      SCHED();
#pragma unroll
      for (int n = 0; n < 16; ++n) {
        if (n < 4) { sacc = mfma_bf(af[n], sel, sacc); qacc[t] = mfma_bf(af[n], af[n], n == 0 ? z4 : qacc[t]); }
        xa[t] = mfma_bf(af[n], Ff[n], n == 0 ? z4 : xa[t]);
        if (t < 3) af[n] = lds128(L, abase[n >> 2] + (n & 3) * 16 + (t + 1) * 16 * XROW);
        if (t < 2 && n == 10) pv = prog_get(L, gL);
        if (t == 3 && n == 6) {
          *(LAS f32x4*)(L + (c < 4 ? LDS_ST + w * 256 + (16 * c + 4 * g) * 4 : LDS_DUMMY + lane * 16)) = sacc;
          const int qo = (c >> 2) == g ? LDS_ST + 1024 + w * 256 + c * 4 : LDS_DUMMY + lane * 4;
#pragma unroll
          for (int tt = 0; tt < 4; ++tt) {
            const float lo = (c & 1) ? qacc[tt][1] : qacc[tt][0], hi = (c & 1) ? qacc[tt][3] : qacc[tt][2];
            *(LAS float*)(L + qo + tt * 64) = (c & 2) ? hi : lo;
          }
          prog_set(L, w, lane, PC_STAT);
        }
        SCHED();
      }
    }
  }
  STAMP(3);
  prog_wait(L, gC, PC_STAT);
  {
    float sm = 0.f, sq = 0.f;
#pragma unroll
    for (int a = 0; a < 4; ++a) { sm += *(LAS float*)(L + LDS_ST + a * 256 + lane * 4); sq += *(LAS float*)(L + LDS_ST + 1024 + a * 256 + lane * 4); }
    const float mean = sm * (1.f / 512.f);
    const float var = fmaxf(sq * (1.f / 512.f) - mean * mean, 0.f);
    const float sd = rbf(__builtin_amdgcn_sqrtf(rbf(rbf(var) + 1e-5f)));
    *(LAS float*)(L + LDS_ST2 + w * 512 + lane * 4) = rbf(mean);
    *(LAS float*)(L + LDS_ST2 + w * 512 + 256 + lane * 4) = __builtin_amdgcn_rcpf(sd);
    FENCE();
  }
  {
    u32x4 xb[2];
    f32x4 rv[4];
#pragma unroll
    for (int t = 0; t < 4; ++t) rv[t] = *(LAS f32x4*)(L + LDS_ST2 + w * 512 + 256 + (16 * t + 4 * g) * 4);
    if (j == 0 && w == 0) {
      const float mco = c == 0 ? 512.f : 0.f;
#pragma unroll
      for (int t = 0; t < 4; ++t) xa[t] -= mco * *(LAS f32x4*)(L + LDS_ST2 + w * 512 + (16 * t + 4 * g) * 4);
    }
#pragma unroll
    for (int t = 0; t < 4; ++t) {
      const f32x4 v = xa[t] * (rv[t] * 0.03125f);
      xb[t >> 1][2 * (t & 1)] = pk_f16(v[0], v[1]);
      xb[t >> 1][2 * (t & 1) + 1] = pk_f16(v[2], v[3]);
    }
    f32x4 tt[4];
#pragma unroll
    for (int t = 0; t < 4; ++t) tt[t] = mfma_h(Gf[t][0], xb[0], z4);
#pragma unroll
    for (int t = 0; t < 4; ++t) tt[t] = mfma_h(Gf[t][1], xb[1], tt[t]);
#pragma unroll
    for (int t = 0; t < 4; ++t)
#pragma unroll
      for (int sub = 0; sub < 2; ++sub)
        *(LAS u32*)(L + LDS_TT + w * 2048 + (8 * t + 2 * g + sub) * 64 + c * 4) = pk_f16(tt[t][2 * sub], tt[t][2 * sub + 1]);
  }
  FENCE();
  const u32x4 tv0 = lds128(L, LDS_TT + w * 2048 + lane * 16), tv1 = lds128(L, LDS_TT + w * 2048 + 1024 + lane * 16);
  const __amdgpu_buffer_rsrc_t rws = MK_RWS();
  const u32 toff = WS_T + ((j * 32 + (lane >> 2)) * 32 + i) * 256 + w * 64 + (lane & 3) * 16;
  u32x2 tag;
  STAMP(4);
  f32x4 acc[3][5];
  {
    const int xbase = LDS_X + c * XROW + 16 * g * 16;
    int wbA[2], wbB[2];
    wbA[0] = LDS_RING + (4 * g + (c >> 2)) * 352 + (3 * w) * 32 + (c & 3) * 8; wbB[0] = wbA[0] + (w == 3 ? 32 : 64);
    wbA[1] = wbA[0] + 4 * W1CH; wbB[1] = wbB[0] + 4 * W1CH;
    pin(wbA[0]); pin(wbA[1]); pin(wbB[0]); pin(wbB[1]);
    u32x4 xf[2][5]; u32x2 wl[2][3], wh[2][3];
    xf[0][4] = u32x4{0x3f803f80u, 0x3f803f80u, 0x3f803f80u, 0x3f803f80u}; pin(xf[0][4]); xf[1][4] = xf[0][4];
#define RD1(n, b, r) do { const int sl_ = ((n) + 5) % NSLOT, so_ = (sl_ & 3) * W1CH, wbase = wbA[sl_ >> 2], wbase2 = wbB[sl_ >> 2]; \
      if ((r) == 0) wl[b][0] = ldstr(L, wbase + so_); else if ((r) == 1) wh[b][0] = ldstr(L, wbase + so_ + 16 * 352); \
      else if ((r) < 6) xf[b][(r) - 2] = lds128(L, xo + ((r) - 2) * 16 * XROW); \
      else if ((r) == 6) wl[b][1] = ldstr(L, wbase + so_ + 32); else if ((r) == 7) wh[b][1] = ldstr(L, wbase + so_ + 32 + 16 * 352); \
      else if ((r) == 8) wl[b][2] = ldstr(L, wbase2 + so_); else wh[b][2] = ldstr(L, wbase2 + so_ + 16 * 352); } while (0)
    prog_wait(L, gP0, 1);
    prog_wait(L, gL, PL_W1 + 1);
    {
      const int xo = xbase + rot1 * 16;
#pragma unroll
      for (int r = 0; r < 10; ++r) RD1(0, 0, r);
    }
    prog_set(L, w, lane, PC_W1);
    bool bad = false;
#pragma unroll
    for (int n = 0; n < 16; ++n) {
      if (__builtin_expect(bad, 0)) prog_spin(L, gL, PL_W1 + n + 1);
      const int xo = xbase + ((n + 1 + rot1) & 15) * 16;
      u32 pv = 0;
      SCHED();
#pragma unroll
      for (int q = 0; q < 15; ++q) {
        const int u = q / 5, t = q % 5;
        const u32x4 wa = {wl[n & 1][u][0], wl[n & 1][u][1], wh[n & 1][u][0], wh[n & 1][u][1]};
        acc[u][t] = mfma_bf(wa, xf[n & 1][t], n == 0 ? z4 : acc[u][t]);
        if (n + 1 < 16) {
          if (q < 10) RD1(n + 1, (n + 1) & 1, q);
          if (q == 10) prog_set(L, w, lane, PC_W1 + n + 1);
          if (q == 7 && n + 2 < 16) pv = prog_get(L, gL);
          if (q == 13 && n + 2 < 16) bad = prog_bad(pv, PL_W1 + n + 2);
          if (n == 0 && q == 1) st128(tv0, rws, toff);
          if (n == 0 && q == 3) st128(tv1, rws, toff + 16 * 8192);
          if (n == TFSTEP - 1 && q == 11) tag = *(LAS u32x2*)(L + LDS_TAG + lane * 8);
          if (n == TFSTEP && q == 3) { WAITV(0); st_flag(tag, rws, WS_FT + ((j * 4 + w) * 32 + i) * 128, lane); }
        }
        SCHED();
      }
      if (n + 2 >= 16) bad = false;
    }
  }
  STAMP(5);
  {
    const int hlo = WS_H + (i * 4 + (((g & 1) + rotx) & 3)) * 43 * 1024 + ((g >> 1) * 16 + c) * 16;
    const int hhi = WS_H + (i * 4 + (((g & 1) + 2 + rotx) & 3)) * 43 * 1024 + ((g >> 1) * 16 + c) * 16;
    float mrow[4], rrow[4], r2row[4];
#pragma unroll
    for (int t = 0; t < 4; ++t) {
      mrow[t] = -*(LAS float*)(L + LDS_ST2 + w * 512 + (16 * t + c) * 4);
      rrow[t] = *(LAS float*)(L + LDS_ST2 + w * 512 + 256 + (16 * t + c) * 4);
      r2row[t] = rrow[t] * -1.4426950408889634f;
    }
#pragma unroll
    for (int u = 0; u < 3; ++u) {
      if (3 * w + u < nt) {
        u32 hp[4][2];
        {
          f32x2 tv[8], ar[8];
#pragma unroll
          for (int e = 0; e < 8; ++e) {
            const int t = e >> 1, hh = e & 1;
            tv[e] = f32x2{acc[u][4][2 * hh], acc[u][4][2 * hh + 1]} * mrow[t] + f32x2{acc[u][t][2 * hh], acc[u][t][2 * hh + 1]};
          }
          SCHED();
#pragma unroll
          for (int e = 0; e < 8; ++e) ar[e] = tv[e] * r2row[e >> 1];
          SCHED();
#pragma unroll
          for (int e = 0; e < 8; ++e) { ar[e] = f32x2{__builtin_amdgcn_exp2f(ar[e][0]), __builtin_amdgcn_exp2f(ar[e][1])}; tv[e] *= rrow[e >> 1]; }
          SCHED();
#pragma unroll
          for (int e = 0; e < 8; ++e) ar[e] += 1.f;
          SCHED();
#pragma unroll
          for (int e = 0; e < 8; ++e) ar[e] = f32x2{__builtin_amdgcn_rcpf(ar[e][0]), __builtin_amdgcn_rcpf(ar[e][1])};
          SCHED();
#pragma unroll
          for (int e = 0; e < 8; ++e) tv[e] *= ar[e];
          SCHED();
#pragma unroll
          for (int e = 0; e < 8; ++e) hp[e >> 1][e & 1] = pk_bf16v(tv[e]);
        }
#pragma unroll
        for (int tp = 0; tp < 2; ++tp) {
          auto a = __builtin_amdgcn_permlane16_swap(hp[2 * tp][0], hp[2 * tp + 1][0], false, false);
          auto b = __builtin_amdgcn_permlane16_swap(hp[2 * tp][1], hp[2 * tp + 1][1], false, false);
          const u32x4 o = {a[0], b[0], a[1], b[1]};
          st128(o, rws, (tp ? hhi : hlo) + (u0 + 3 * w + u) * 512);
        }
      }
    }
  }
  STAMPX(6);
  const int orow = 32 * (16 * w + c) + i3, rotA = 2 * j3;
  {
    const float st = (float)(2 * (64 * j3 + 16 * w + c) + 1) * (1.f / 1024.f);
    const float K = 2.f * cosrev(32.f * st);
    int fa = LDS_FI + w * 1024 + lane * 16; pin(fa);
#pragma unroll
    for (int reim = 0; reim < 2; ++reim) {
      u32x4 fo[8];
#pragma unroll
      for (int r = 0; r < 4; ++r) {
        const float a0 = (float)(16 * (g >> 1) + (g & 1) + 4 * r) * st + (reim ? 0.25f : 0.f), a1 = a0 + 32.f * st;
        f32x2 pa = {cosrev(a0), cosrev(a0 + 2.f * st)}, pb = {cosrev(a1), cosrev(a1 + 2.f * st)};
        f32x2 p0 = pa;
        if (reim == 1 && r == 0 && g == 0) p0[0] = (c & 1) ? -1.f : 1.f;
        fo[0][r] = pk_bf16v(p0); fo[1][r] = pk_bf16v(pb);
#pragma unroll
        for (int jk = 2; jk < 8; ++jk) { const f32x2 pc = K * pb - pa; pa = pb; pb = pc; fo[jk][r] = pk_bf16v(pc); }
      }
#pragma unroll
      for (int jk = 0; jk < 8; ++jk) pin(fo[jk]);
      if (reim == 0) prog_wait(L, gC, PC_W1 + 15);
#pragma unroll
      for (int jk = 0; jk < 8; ++jk) *(LAS u32x4*)(L + fa + ((reim * 8 + jk - rotA) & 15) * 4096) = fo[jk];
      if (reim == 0) { WAITV(0); st_flag(tag, rws, WS_FH + ((i * 8 + j) * 4 + w) * 128, lane); prog_set(L, w, lane, PC_H); }
    }
  }
  prog_set(L, w, lane, PC_FI);
  STAMPX(7);
  u32x4 gi[2];
  {
    const float sf = (float)(16 * w + c) * (1.f / 64.f);
#pragma unroll
    for (int kp = 0; kp < 2; ++kp)
#pragma unroll
      for (int r = 0; r < 4; ++r) {
        const float a = (float)(16 * kp + 4 * g + r) * sf;
        float hi = cosrev(a - 0.25f);
        if (kp == 0 && r == 0 && g == 0) hi = (c & 1) ? -1.f : 1.f;
        gi[kp][r] = pk_bf16(cosrev(a), hi);
      }
    pin(gi[0]); pin(gi[1]);
  }
  u32x2 xv[4];
#pragma unroll
  for (int nu = 0; nu < 4; ++nu) xv[nu] = *(const GAS u32x2*)(x + orow * D_ + 64 * j3 + 16 * nu + 4 * g);
  const u32 aoff = WS_A + (i3 * 4 + w) * 16384 + lane * 16;
  u32x4 af[16];
#define A_LOAD(ks, vo) af[ks] = __builtin_amdgcn_raw_buffer_load_b128(rws, vo, (((ks) + rotA) & 15) * 1024, SC1)
#define PM_BLOCK(NPRE) do { \
    prog_wait(L, gC, PC_FI); prog_wait(L, gP, 4); \
    f32x4 pm[4]; u32x4 fi[12]; \
    _Pragma("unroll") for (int n = 0; n < 12; ++n) fi[n] = lds128(L, LDS_FI + n * 1024 + lane * 16); \
    SCHED(); \
    _Pragma("unroll") for (int n = 0; n < 64; ++n) { \
      pm[n & 3] = mfma_bf(af[n >> 2], fi[n % 12], n < 4 ? z4 : pm[n & 3]); \
      if (n + 12 < 64) fi[n % 12] = lds128(L, LDS_FI + (n + 12) * 1024 + lane * 16); \
      if ((n & 3) == 1 && (n >> 2) + (NPRE) < 16) A_LOAD((n >> 2) + (NPRE), aoff); \
      SCHED(); \
    } \
    _Pragma("unroll") for (int nu = 0; nu < 4; ++nu) \
      *(LAS u32x2*)(L + LDS_PM + (16 * nu + c) * PMROW + (16 * w + 4 * g) * 2) = u32x2{pk_bf16(pm[nu][0], pm[nu][1]), pk_bf16(pm[nu][2], pm[nu][3])}; \
    FENCE(); *(volatile LAS u32*)(L + LDS_PROG2 + w * 256 + lane * 4) = 1u; FENCE(); \
  } while (0)
  bool pm_done = false;
  {
    const int gP2 = gP;
    u64 rdy = 0;
    NOUNROLL
    for (int sp = 0; sp < SPIN_CAP * 8; ++sp) { rdy = __builtin_amdgcn_ballot_w64(prog_get(L, gP2) >= 5u); if (rdy) break; }
    FENCE();
    STAMPX(8);
    if (!(rdy & 2)) {
#pragma unroll
      for (int ks = 0; ks < 8; ++ks) { A_LOAD(ks, aoff); SCHED(); }
      PM_BLOCK(8);
      pm_done = true;
    }
  }
  STAMPX(9);
  f32x4 oa[4], ob[4];
  {
    const int kh = w & 1;
    constexpr int HW = 6;
    u32x4 ha[HW], hb[HW];
    const int hoa = WS_H + (i3 * 4 + w) * 43 * 1024 + lane * 16, hob = WS_H + (i3 * 4 + (w ^ 1)) * 43 * 1024 + lane * 16;
    u32 hso = (kh + rot2) * 1024; if (hso >= 43 * 1024) hso -= 43 * 1024;
#define HNEXT() do { hso += 2048; if (hso >= 43 * 1024) hso -= 43 * 1024; } while (0)
    int wb1[4], wb2[4];
    constexpr int WB1 = LDS_RING, WB2 = LDS_RING + 7 * 8192;
    {
      const int row = 8 * g + (c >> 2), swz = ((row >> 1) & 1) | (((row >> 3) & 1) << 1);
#pragma unroll
      for (int nu = 0; nu < 4; ++nu) {
        wb1[nu] = WB1 + kh * 4096 + row * 128 + ((nu ^ swz) * 32) + (c & 3) * 8; wb2[nu] = wb1[nu] + (WB2 - WB1);
        pin(wb1[nu]); pin(wb2[nu]);
      }
    }
#define W2ADDR(nu, un) (u2slot(un) >= WB2 ? wb2[nu] + (u2slot(un) - WB2) : wb1[nu] + (u2slot(un) - WB1))
    u32x2 wf[2][8];
    prog_wait(L, gL, PL_W2 + U2N - 1);
#pragma unroll
    for (int r = 0; r < 8; ++r) wf[0][r] = ldstr(L, W2ADDR(r >> 1, 0) + (r & 1) * 512);
    prog_set(L, w, lane, PC_W2);
    prog_wait(L, gP1, 5);
    STAMPX(10);
#pragma unroll
    for (int n = 0; n < HW; ++n) {
      ha[n] = __builtin_amdgcn_raw_buffer_load_b128(rws, hoa, hso, X_HAUX); SCHED();
      hb[n] = __builtin_amdgcn_raw_buffer_load_b128(rws, hob, hso, X_HAUX);
      HNEXT(); SCHED();
    }
    bool bad = false;
    u32 avo = OOB;
#pragma unroll
    for (int un = 0; un < 21; ++un) {
      if (__builtin_expect(bad, 0)) prog_spin(L, gL, PL_W2 + un + 1);
      if (un == 22 - HW && !pm_done) { prog_wait(L, gP0, 5); avo = aoff; }
      u32 pv = 0;
      SCHED();
#pragma unroll
      for (int q = 0; q < 8; ++q) {
        const int nu = q >> 1;
        const u32x4 wa = {wf[un & 1][2 * nu][0], wf[un & 1][2 * nu][1], wf[un & 1][2 * nu + 1][0], wf[un & 1][2 * nu + 1][1]};
        if (q & 1) ob[nu] = mfma_bf(wa, hb[un % HW], un == 0 ? z4 : ob[nu]); else oa[nu] = mfma_bf(wa, ha[un % HW], un == 0 ? z4 : oa[nu]);
        if (q == 0 && un + 2 < 22 && un + 2 >= U2N) pv = prog_get(L, gL);
        if (q < 4) { wf[(un + 1) & 1][2 * q] = ldstr(L, W2ADDR(q, un + 1)); wf[(un + 1) & 1][2 * q + 1] = ldstr(L, W2ADDR(q, un + 1) + 512); }
        if (q == 4 && un + 1 + U2N < 22) prog_set(L, w, lane, PC_W2 + un + 1);
        if (q == 5 && un + 2 < 22 && un + 2 >= U2N) bad = prog_bad(pv, PL_W2 + un + 2);
        if (un + HW < 22) {
          if (q == 6) ha[un % HW] = __builtin_amdgcn_raw_buffer_load_b128(rws, hoa, hso, X_HAUX);
          if (q == 7) { hb[un % HW] = __builtin_amdgcn_raw_buffer_load_b128(rws, hob, hso, X_HAUX); HNEXT(); }
        } else {
          if (q == 6) A_LOAD(2 * (un + HW - 22), avo);
          if (q == 7) A_LOAD(2 * (un + HW - 22) + 1, avo);
        }
        SCHED();
      }
      if (un + 2 >= 22) bad = false;
    }
    if (kh == 0) {
#pragma unroll
      for (int nu = 0; nu < 4; ++nu) {
        const u32x4 wa = {wf[1][2 * nu][0], wf[1][2 * nu][1], wf[1][2 * nu + 1][0], wf[1][2 * nu + 1][1]};
        oa[nu] = mfma_bf(wa, ha[21 % HW], oa[nu]); ob[nu] = mfma_bf(wa, hb[21 % HW], ob[nu]);
      }
    }
    STAMPX(11);
    if (!pm_done) PM_BLOCK(2 * (HW - 1));
  }
  {
    int g2 = LDS_PROG2 + (lane & 3) * 260; pin(g2);
    prog_wait(L, g2, 1);
  }
#pragma unroll
  for (int nu = 0; nu < 4; ++nu) *(LAS f32x4*)(L + LDS_RED + ((w ^ 1) * 4 + nu) * 1024 + lane * 16) = ob[nu];
  prog_set(L, w, lane, PC_RED);
  {
    u32x4 pf[8];
#pragma unroll
    for (int n = 0; n < 8; ++n) pf[n] = lds128(L, LDS_PM + (16 * (n >> 1) + c) * PMROW + (32 * (n & 1) + 8 * g) * 2);
#pragma unroll
    for (int nu = 0; nu < 4; ++nu) oa[nu] = mfma_bf(pf[2 * nu], gi[0], oa[nu]);
#pragma unroll
    for (int nu = 0; nu < 4; ++nu) oa[nu] = mfma_bf(pf[2 * nu + 1], gi[1], oa[nu]);
  }
  prog_wait(L, pga_of(w ^ 1, 1, lane), PC_RED);
#pragma unroll
  for (int nu = 0; nu < 4; ++nu) {
    const int col = 64 * j3 + 16 * nu + 4 * g;
    const f32x4 v = oa[nu] + *(LAS f32x4*)(L + LDS_RED + (w * 4 + nu) * 1024 + lane * 16);
    const u32x2 o = {pk_bf16(v[0] + bf2f(xv[nu][0] & 0xffffu), v[1] + asf(xv[nu][0] & 0xffff0000u)),
                     pk_bf16(v[2] + bf2f(xv[nu][1] & 0xffffu), v[3] + asf(xv[nu][1] & 0xffff0000u))};
    *(GAS u32x2*)(out + orow * D_ + col) = o;
  }
  STAMPX(12);
  STAMPX(13);
  STAMP_OUT();
}

extern "C" void kernel_launch(void* const* d_in, const int* in_sizes, int n_in, void* d_out, int out_size, void* d_ws, size_t ws_size,
                              hipStream_t stream) {
  const u16* x = (const u16*)d_in[0]; const u16* wr = (const u16*)d_in[1]; const u16* wi = (const u16*)d_in[2];
  const u16* w1 = (const u16*)d_in[4]; const u16* w2 = (const u16*)d_in[5];
  (void)hipFuncSetAttribute((const void*)k_all, hipFuncAttributeMaxDynamicSharedMemorySize, LDS_BYTES);
  k_all<<<256, 512, LDS_BYTES, stream>>>(x, wr, wi, w1, w2, (unsigned char*)d_ws, (u16*)d_out);
}
```

```cpp
#include <hip/hip_runtime.h>
#include <hip/hip_bf16.h>
#define MAP84

typedef unsigned u32;
typedef unsigned short u16;
typedef unsigned long long u64;
typedef u32 u32x4 __attribute__((ext_vector_type(4)));
typedef u32 u32x2 __attribute__((ext_vector_type(2)));
typedef float f32x4 __attribute__((ext_vector_type(4)));
typedef float f32x2 __attribute__((ext_vector_type(2)));
typedef __bf16 bf16x8 __attribute__((ext_vector_type(8)));
typedef __bf16 bf16x2 __attribute__((ext_vector_type(2)));
typedef _Float16 f16x8 __attribute__((ext_vector_type(8)));
typedef _Float16 f16x2 __attribute__((ext_vector_type(2)));
typedef short s16x4 __attribute__((ext_vector_type(4)));
#define LAS __attribute__((address_space(3)))
#define GAS __attribute__((address_space(1)))
#define DEVI __device__ __forceinline__

constexpr int S_ = 2048, D_ = 512, F_ = 1376, DF = 257;
constexpr u32 WS_EPOCH = 0;
constexpr u32 WS_CV = 65536;
constexpr u32 WS_DV = 65536 + 8192;
constexpr u32 WS_FT = 131072;
constexpr u32 WS_FH = 262144;
constexpr u32 WS_T = 393216;
constexpr u32 WS_H = WS_T + (2u << 20);
constexpr u32 WS_A = WS_H + 32u * 4 * 43 * 1024;
constexpr u32 WS_END = WS_A + (2u << 20);
constexpr int XROW = 1040;
constexpr int LDS_X = 0;
constexpr int W1CH = 11264;
constexpr int NSLOT = 7;
constexpr int LDS_RING = 64 * XROW;
constexpr int LDS_TT = LDS_RING + NSLOT * W1CH;
constexpr int LDS_ST = LDS_TT + 8192;
constexpr int LDS_ST2 = LDS_ST + 2048;
constexpr int LDS_PROG = LDS_ST2 + 2048;
constexpr int LDS_TAG = LDS_PROG + 2048;
constexpr int LDS_DUMMY = LDS_TAG + 512;
constexpr int LDS_BYTES = 163840;
constexpr int LDS_FI = 0;
constexpr int U2N = 9;
constexpr int LDS_PM = LDS_TT, PMROW = 144;
constexpr int LDS_RED = 0;
constexpr int LDS_PROG2 = LDS_DUMMY + 1024;
DEVI constexpr int u2slot(int n) { return LDS_RING + (n % U2N) * 8192; }
constexpr int PC_STAT = 1, PC_W1 = 3  , PC_FI = 21, PC_HS = 31, PC_RED = 50;
DEVI constexpr int pcw2(int n) { return n < 9 ? 22 + n : 23 + n; }
constexpr int PL_W1 = 5, PL_W2 = 21;
constexpr int SPIN_CAP = 1 << 15;
constexpr u32 SENT = 0xffffffffu;

DEVI float asf(u32 v) { return __builtin_bit_cast(float, v); }
DEVI u32 asu(float v) { return __builtin_bit_cast(u32, v); }
DEVI float cosrev(float x) { return __builtin_amdgcn_cosf(x); }
DEVI u32 pk_bf16(float a, float b) { f32x2 v = {a, b}; return __builtin_bit_cast(u32, __builtin_convertvector(v, bf16x2)); }
DEVI u32 pk_f16(float a, float b) { f32x2 v = {a, b}; return __builtin_bit_cast(u32, __builtin_convertvector(v, f16x2)); }
DEVI u32 pk_bf16v(f32x2 v) { return __builtin_bit_cast(u32, __builtin_convertvector(v, bf16x2)); }
DEVI u32 pk_f16v(f32x2 v) { return __builtin_bit_cast(u32, __builtin_convertvector(v, f16x2)); }
DEVI float rbf(float a) { return asf(pk_bf16(a, a) << 16); }
DEVI float bf2f(u32 h) { return asf(h << 16); }
DEVI f32x4 mfma_bf(u32x4 a, u32x4 b, f32x4 c) {
  return __builtin_amdgcn_mfma_f32_16x16x32_bf16(__builtin_bit_cast(bf16x8, a), __builtin_bit_cast(bf16x8, b), c, 0, 0, 0);
}
DEVI f32x4 mfma_h(u32x4 a, u32x4 b, f32x4 c) {
  return __builtin_amdgcn_mfma_f32_16x16x32_f16(__builtin_bit_cast(f16x8, a), __builtin_bit_cast(f16x8, b), c, 0, 0, 0);
}
DEVI void mfa(f32x4& c, u32x4 a, u32x4 b) { asm volatile("v_mfma_f32_16x16x32_bf16 %0, %1, %2, %0" : "+v"(c) : "v"(a), "v"(b)); }
DEVI void mfz(f32x4& c, u32x4 a, u32x4 b) { asm volatile("v_mfma_f32_16x16x32_bf16 %0, %1, %2, 0" : "=&v"(c) : "v"(a), "v"(b)); }
#define MFA(first, c, a, b) do { if (first) mfz(c, a, b); else mfa(c, a, b); } while (0)
#define MFMA_SETTLE() asm volatile("s_nop 7\n\ts_nop 1" ::: "memory")
DEVI float dot2bf(u32 a, u32 b, float c) {
  return __builtin_amdgcn_fdot2_f32_bf16(__builtin_bit_cast(bf16x2, a), __builtin_bit_cast(bf16x2, b), c, false);
}
DEVI u32x4 mk_srd(const void* p, u32 bytes) {
  u64 a = (u64)p;
  u32x4 r = {(u32)a, (u32)(a >> 32) & 0xffffu, bytes, 0x00020000u};
  return r;
}
DEVI void dma16(u32x4 srd, u32 voff, u32 soff, u32 lds) {
  asm volatile("s_mov_b32 m0, %3\n\ts_nop 0\n\tbuffer_load_dwordx4 %0, %1, %2 offen lds"
               :: "v"(voff), "s"(srd), "s"(soff), "s"(lds) : "memory");
}
DEVI void dma_x8(u32x4 srd, u32 voff, u32 soff, u32 lds) {
  u32 t;
#define DX_ "s_add_u32 m0, m0, 0x410\n\ts_add_u32 %0, %0, 0x8000\n\tbuffer_load_dwordx4 %1, %2, %0 offen lds\n\t"
  asm volatile("s_mov_b32 m0, %4\n\ts_mov_b32 %0, %3\n\tbuffer_load_dwordx4 %1, %2, %0 offen lds\n\t" DX_ DX_ DX_ DX_ DX_ DX_ DX_
               : "=&s"(t) : "v"(voff), "s"(srd), "s"(soff), "s"(lds) : "memory", "scc");
#undef DX_
}
DEVI void dma_4k(u32x4 srd, u32 v0, u32 v1, u32 v2, u32 v3, u32 soff, u32 lds) {
  asm volatile("s_mov_b32 m0, %6\n\ts_nop 0\n\tbuffer_load_dwordx4 %0, %4, %5 offen lds\n\tbuffer_load_dwordx4 %1, %4, %5 offen offset:1024 lds\n\t"
               "buffer_load_dwordx4 %2, %4, %5 offen offset:2048 lds\n\tbuffer_load_dwordx4 %3, %4, %5 offen offset:3072 lds"
               :: "v"(v0), "v"(v1), "v"(v2), "v"(v3), "s"(srd), "s"(soff), "s"(lds) : "memory");
}
DEVI void dma_w1tail(u32x4 srd, u32 v4, u32 v5, u32 soff) {
  asm volatile("s_add_u32 m0, m0, 0x1000\n\ts_nop 0\n\tbuffer_load_dwordx4 %0, %2, %3 offen lds\n\ts_mov_b32 exec_hi, 0\n\t"
               "buffer_load_dwordx4 %1, %2, %3 offen offset:1024 lds\n\ts_mov_b32 exec_hi, -1"
               :: "v"(v4), "v"(v5), "s"(srd), "s"(soff) : "memory", "scc");
}
DEVI void touch(u32x4 srd, u32 voff, u32 lds) {
  asm volatile("s_mov_b32 m0, %2\n\ts_nop 0\n\tbuffer_load_dword %0, %1, 0 offen lds" :: "v"(voff), "s"(srd), "s"(lds) : "memory");
}
#define WAITV_(n) asm volatile("s_waitcnt vmcnt(" #n ")" ::: "memory")
#define WAITV(n) WAITV_(n)
#define W2WIN 16
#define WAITL() asm volatile("s_waitcnt lgkmcnt(0)" ::: "memory")
#define FENCE() asm volatile("" ::: "memory")
#define SCHED() __builtin_amdgcn_sched_barrier(0)
#define BAR0() do { asm volatile("s_waitcnt lgkmcnt(0)\n\ts_barrier" ::: "memory"); SCHED(); } while (0)
#define NOUNROLL _Pragma("unroll 1")
DEVI u32x4 lds128(LAS unsigned char* L, int off) { return *(LAS u32x4*)(L + off); }
DEVI u32x2 ldstr(LAS unsigned char* L, int off) {
  return __builtin_bit_cast(u32x2, __builtin_amdgcn_ds_read_tr16_b64_v4i16((LAS s16x4*)(L + off)));
}
DEVI float ror8(float v) { return asf(__builtin_amdgcn_mov_dpp((int)asu(v), 0x128, 0xf, 0xf, true)); }
constexpr int SC1 = 16;
constexpr u32 OOB = 0x7ffffff0u;
DEVI void st128(u32x4 v, __amdgpu_buffer_rsrc_t rs, u32 off) { __builtin_amdgcn_raw_buffer_store_b128(v, rs, off, 0, SC1); }
DEVI void st_flag(u32x2 tag, __amdgpu_buffer_rsrc_t rs, u32 off, int lane) { __builtin_amdgcn_raw_buffer_store_b64(tag, rs, lane == 0 ? off : OOB, 0, SC1); }
#define MK_RWS() __builtin_amdgcn_make_buffer_rsrc((void*)ws, 0, (int)WS_END, 0x00020000)
template <int V> struct IC { static constexpr int value = V; };
template <int I, int N, class F> DEVI void sfor(F&& f) { if constexpr (I < N) { f(IC<I>{}); sfor<I + 1, N>(f); } }
#define AINL __attribute__((always_inline))
template <class T> DEVI void pin(T& v) { asm volatile("" : "+v"(v)); }
DEVI float sfma(float a, float b, float c) { float r = __builtin_fmaf(a, b, c); asm("" : "+v"(r)); return r; }
DEVI float smul(float a, float b) { float r = a * b; asm("" : "+v"(r)); return r; }
DEVI float sadd(float a, float b) { float r = a + b; asm("" : "+v"(r)); return r; }
#define SGB(mask, n) __builtin_amdgcn_sched_group_barrier(mask, n, 0)
DEVI u32 umax(u32 a, u32 b) { return a > b ? a : b; }
DEVI u32 max4(u32x4 v) { return umax(umax(v[0], v[1]), umax(v[2], v[3])); }
DEVI bool any_sent(u32 m) { return __builtin_amdgcn_ballot_w64(m == SENT) != 0ull; }

#define STAMP(n)
#define STAMP_DECL
#define STAMP_OUT()

#define STAMPX(n) STAMP(n)
DEVI void prog_set(LAS unsigned char* L, int w, int lane, u32 v) {
  FENCE(); *(volatile LAS u32*)(L + LDS_PROG + w * 256 + lane * 4) = v; FENCE();
}
DEVI int pga_of(int first, int cnt, int lane) { int a = LDS_PROG + (first + (lane & (cnt - 1))) * 260; pin(a); return a; }
DEVI u32 prog_get(LAS unsigned char* L, int pga) { return *(volatile LAS u32*)(L + pga); }
DEVI bool prog_bad(u32 v, u32 target) { return __builtin_expect(__builtin_amdgcn_ballot_w64(v < target) != 0ull, 0); }
template <int SL = 0> DEVI void prog_spin(LAS unsigned char* L, int pga, u32 target) {
  NOUNROLL
  for (int s = 0; s < SPIN_CAP * 8; ++s) { if (SL) __builtin_amdgcn_s_sleep(SL); if (__builtin_amdgcn_ballot_w64(prog_get(L, pga) < target) == 0ull) break; }
  FENCE();
}
template <int SL = 0> DEVI void prog_wait(LAS unsigned char* L, int pga, u32 target) {
  if (prog_bad(prog_get(L, pga), target)) prog_spin<SL>(L, pga, target);
  FENCE();
}
template <int NL, int SL> DEVI void flag_wait(__amdgpu_buffer_rsrc_t rs, u32 off, int lane, int nlanes, u32x2 tag) {
  bool miss[NL];
#pragma unroll
  for (int n = 0; n < NL; ++n) miss[n] = lane < nlanes;
  NOUNROLL
  for (int s = 0; s < SPIN_CAP; ++s) {
    FENCE();
    u32x2 f[NL];
#pragma unroll
    for (int n = 0; n < NL; ++n) f[n] = __builtin_amdgcn_raw_buffer_load_b64(rs, miss[n] ? off + (n * 64 + lane) * 128 : 0x7ffffff0u, 0, 16);
    bool any = false;
#pragma unroll
    for (int n = 0; n < NL; ++n) { miss[n] = miss[n] && !(f[n][0] == tag[0] && f[n][1] == tag[1]); any |= miss[n]; }
    if (__builtin_amdgcn_ballot_w64(any) == 0ull) break;
    __builtin_amdgcn_s_sleep(SL);
  }
  FENCE();
}

DEVI void gen32(float a0, float step, float K, u32x4* o) {
  f32x2 p = {cosrev(a0), cosrev(a0 + step)}, q = {cosrev(a0 + 2.f * step), cosrev(a0 + 3.f * step)};
  o[0][0] = pk_bf16v(p); o[0][1] = pk_bf16v(q);
#pragma unroll
  for (int m = 2; m < 16; ++m) { const f32x2 r = K * q - p; p = q; q = r; o[m >> 2][m & 3] = pk_bf16v(r); }
}

#define TFSTEP 5
#define CODE_LINES 390
#define TPOLL 9
#define W2STOP 3
#define WLOADAT 3
#define J3SH 0
#define TSLEEP 8

#define BID __builtin_amdgcn_workgroup_id_x()
#define TID __builtin_amdgcn_workitem_id_x()
struct Args { const u16* x; const u16* wr; const u16* wi; const u16* w1; const u16* w2; unsigned char* ws; u16* out; };
#define CAS __attribute__((address_space(4)))
__global__ __launch_bounds__(512, 2) void k_all(Args args_unused) {
  extern __shared__ __attribute__((aligned(16))) unsigned char smem[];
  LAS unsigned char* L = (LAS unsigned char*)smem;
  const int tid = TID, lane = tid & 63, w = __builtin_amdgcn_readfirstlane(tid >> 6), g = lane >> 4, c = lane & 15;
  u64 pc0; asm volatile("s_getpc_b64 %0" : "=s"(pc0));
  STAMP_DECL STAMP(0);
  *(volatile LAS u32*)(L + LDS_PROG + tid * 4) = 0u;
  BAR0();
  const CAS Args* ap = (const CAS Args*)__builtin_amdgcn_kernarg_segment_ptr();
  asm volatile("" : "+s"(ap));
#define x (ap->x)
#define wr (ap->wr)
#define wi (ap->wi)
#define w1 (ap->w1)
#define w2 (ap->w2)
#define ws (ap->ws)
#define out (ap->out)
  const int xc = BID & 7, idx = BID >> 3;
  const int i = 8 * (xc >> 1) + (idx >> 2), j = 4 * (xc & 1) + (idx & 3);
  const int i3 = i, j3 = j;
  const int rot2 = (11 * (idx & 3) + 5 * (idx >> 2)) % 43, rotA = 4 * (idx & 3);
  const int rotx = idx & 3, rot1 = 2 * (idx >> 2);
  const int u0 = 11 * j - (j == 7), nt = 11 - (j >= 6);

  if (__builtin_expect(w >= 4 && w < 6, 1)) {
    __builtin_amdgcn_s_setprio(3);
    const int l = w - 4;
    const u32 lbase = (u32)(size_t)L;
    const u32x4 sx = mk_srd(x, S_ * D_ * 2), sw1 = mk_srd((const char*)w1 - 4096, D_ * F_ * 2 + 4096), sw2 = mk_srd(w2, D_ * F_ * 2);
#define X_ISSUE(t) dma_x8(sx, lane * 16, (32 * ((16 * (t) + 8 * l + 16 * rotx) & 63) + i) * 1024, lbase + LDS_X + (16 * (t) + 8 * l) * XROW)
    X_ISSUE(0); X_ISSUE(1); X_ISSUE(2);
    WAITV(16); prog_set(L, w, lane, 1);
    X_ISSUE(3);
    WAITV(16); prog_set(L, w, lane, 2);
    SCHED();
    u32 w1off[6];
#pragma unroll
    for (int n = 0; n < 6; ++n) {
      const int e = n * 64 + lane, rr = (e * 745) >> 14; int ch = e - rr * 22;
      if (ch >= 2 * nt) ch -= 2;
      w1off[n] = (rr & 3) * (F_ * 2) + ((rr >> 2) & 3) * (128 * F_ * 2) + ch * 16 + (4 * l * (F_ * 2) + u0 * 32 + 4096 - (n & 3) * 1024);
      pin(w1off[n]);
    }
    SCHED();
    WAITV(8); prog_set(L, w, lane, 3);
    STAMP(1);
    const int gC = pga_of(0, 4, lane);
    {
      u32 kk = rot1, pv = 0;
      const u32 d0 = lbase + LDS_RING + l * 5632;
      u32 w2off[4];
      const __amdgpu_buffer_rsrc_t rw2 = __builtin_amdgcn_make_buffer_rsrc((void*)w2, 0, D_ * F_ * 2, 0x00020000);
      u32x4 stg[52];
      int mdone = 0;
      u32 so2 = (rot2 + l + 2 * U2N) * (32 * D_ * 2); if (so2 >= 43 * 32 * D_ * 2) so2 -= 43 * 32 * D_ * 2;
#define W2STG(m) do { _Pragma("unroll") for (int q = 0; q < 4; ++q) stg[(m) * 4 + q] = __builtin_amdgcn_raw_buffer_load_b128(rw2, w2off[q] + q * 1024, so2, 0); \
        so2 += 2 * 32 * D_ * 2; if (so2 >= 43 * 32 * D_ * 2) so2 -= 43 * 32 * D_ * 2; } while (0)
#pragma unroll
      for (int n = 0; n < 16; ++n) {
        if (n >= NSLOT) { if (prog_bad(pv, PC_W1 + (n - NSLOT))) prog_spin<1>(L, gC, PC_W1 + (n - NSLOT)); }
        const u32 so = kk * (8 * F_ * 2);
        dma_4k(sw1, w1off[0], w1off[1], w1off[2], w1off[3], so, d0 + ((n + 5) % NSLOT) * W1CH);
        kk = (kk + 1) & 15;
        dma_w1tail(sw1, w1off[4], w1off[5], so);
        if (n >= NSLOT && n + 1 < 16) pv = prog_get(L, gC);
        if (n == 0) { WAITV(6); prog_set(L, w, lane, 4); STAMP(7); }
        else if (n == 6) { WAITV(12); prog_set(L, w, lane, 3 + n); STAMP(8); }
        else if (n >= 2 && n < NSLOT) { WAITV(12); prog_set(L, w, lane, 3 + n); }
        else if (n >= NSLOT + 3) { WAITV(18); prog_set(L, w, lane, 2 + n); }
        if (n == NSLOT - 1) {
#pragma unroll
          for (int q = 0; q < 4; ++q) {
            const int row = 8 * q + (lane >> 3), pos = lane & 7, swz = ((row >> 1) & 1) | (((row >> 3) & 1) << 1);
            w2off[q] = row * (D_ * 2) + 128 * j3 + ((((pos >> 1) ^ swz) << 1) | (pos & 1)) * 16 - q * 1024;
            pin(w2off[q]);
          }
          WAITV(6); prog_set(L, w, lane, 4 + n);
#pragma unroll
          for (int m = 0; m < 13; ++m) {
            if (mdone == m) {
              pv = prog_get(L, gC);
              W2STG(m);
              if (m == 0) { WAITV(4); prog_set(L, w, lane, 5 + n); } else { WAITV(W2WIN); }
              if (__builtin_amdgcn_ballot_w64(pv < W2STOP) != 0ull) mdone = m + 1; else mdone = m + 101;
            }
          }
          if (mdone > 100) mdone -= 100;
          STAMP(6);
          pv = prog_get(L, gC);
        }
      }
      WAITV(12); prog_set(L, w, lane, PL_W1 + 13);
      WAITV(6); prog_set(L, w, lane, PL_W1 + 14);
      STAMP(2);
      WAITV(0); prog_set(L, w, lane, PL_W1 + 15);
#pragma unroll
      for (int m = 1; m < 13; ++m) if (m >= mdone) { W2STG(m); WAITV(W2WIN); }
      u32 so = (rot2 + l) * (32 * D_ * 2); if (so >= 43 * 32 * D_ * 2) so -= 43 * 32 * D_ * 2;
      const u32 d2 = lbase + l * 4096;
#pragma unroll
      for (int n = 0; n < U2N; ++n) {
        prog_wait<1>(L, gC, PC_W1 + 9 + (n * 8192 + 8191) / W1CH);
        dma_4k(sw2, w2off[0], w2off[1], w2off[2], w2off[3], so, d2 + u2slot(n));
        WAITV(W2WIN);
        so += 2 * 32 * D_ * 2; if (so >= 43 * 32 * D_ * 2) so -= 43 * 32 * D_ * 2;
      }
      STAMP(4);
      WAITV(0); prog_set(L, w, lane, PL_W2 + U2N - 1);
      STAMP(5);
      int wa0 = LDS_RING + l * 4096 + lane * 16, wa1 = wa0 + 4 * 8192; pin(wa0); pin(wa1);
#pragma unroll
      for (int n = U2N; n < 22; ++n) {
        prog_wait<1>(L, gC, pcw2(n - U2N));
#pragma unroll
        for (int q = 0; q < 4; ++q) *(LAS u32x4*)(L + ((n % U2N) < 4 ? wa0 + (n % U2N) * 8192 : wa1 + ((n % U2N) - 4) * 8192) + q * 1024) = stg[(n - U2N) * 4 + q];
        prog_set(L, w, lane, PL_W2 + n);
      }
    }
    STAMP(3);
    STAMP_OUT();
    return;
  }

  if (w >= 6) {
    const int q = w - 6;
    const __amdgpu_buffer_rsrc_t rws = MK_RWS();
    const int p = (i & ~1) + q, hq = i & 1;
    const int gC = pga_of(0, 4, lane), gP = pga_of(6, 2, lane), gP0 = pga_of(6, 1, lane);
    u32x2 ep = {0u, 0u};
    if (q == 0) ep = __builtin_amdgcn_raw_buffer_load_b64(rws, WS_EPOCH, 0, SC1);
    if (q == 1) {
      const int ln = idx * 16 + c;
      if (lane < 16 && ln < CODE_LINES) { const u32 v = *(const volatile GAS u32*)(pc0 + (u64)ln * 128); asm volatile("" :: "v"(v)); }
    }
    u32x2 tag;
    {
      SCHED();
      prog_wait<2>(L, pga_of(4, 2, lane), WLOADAT);
      const bool spec = j == 0 && hq == 0;
      u32 raw[8][4], nyr = 0, nyi = 0;
      {
        const __amdgpu_buffer_rsrc_t rwr = __builtin_amdgcn_make_buffer_rsrc((void*)wr, 0, S_ * DF * 2, 0x00020000);
        const __amdgpu_buffer_rsrc_t rwi = __builtin_amdgcn_make_buffer_rsrc((void*)wi, 0, S_ * DF * 2, 0x00020000);
        const int colb = (32 * j + 16 * hq + c) * 2;
        const int vp = 128 * g * (DF * 2) + colb, vm = (384 - 128 * g) * (DF * 2) + colb;
        const int vm0 = ((p == 0 && g == 0) ? 1024 : 2048 - p - 128 * g) * (DF * 2) + colb;
#define W_LOADS(dst, dc) _Pragma("unroll") for (int e = 0; e < 8; ++e) { \
          const int ku = p + 512 * (e >> 1) + 64 * (e & 1) - ((e >= 4 && p == 0) ? 992 : 0); \
          dst[e][0] = __builtin_amdgcn_raw_buffer_load_b16(rwr, vp + (dc), ku * (DF * 2), 0); \
          dst[e][1] = __builtin_amdgcn_raw_buffer_load_b16(rwi, vp + (dc), ku * (DF * 2), 0); \
          dst[e][2] = __builtin_amdgcn_raw_buffer_load_b16(rwr, (e == 0 ? vm0 : vm) + (dc), e == 0 ? 0 : (1664 - ku) * (DF * 2), 0); \
          dst[e][3] = __builtin_amdgcn_raw_buffer_load_b16(rwi, (e == 0 ? vm0 : vm) + (dc), e == 0 ? 0 : (1664 - ku) * (DF * 2), 0); }
        W_LOADS(raw, 0)
        if (spec) {
          const int e = c >> 1, kp = p + 512 * (e >> 1) + 64 * (e & 1) + 128 * g - ((e >= 4 && p == 0) ? 992 : 0);
          const int kr = (c & 1) ? (kp == 0 ? 1024 : 2048 - kp) : kp;
          nyr = __builtin_amdgcn_raw_buffer_load_b16(rwr, kr * (DF * 2) + 512, 0, 0);
          nyi = __builtin_amdgcn_raw_buffer_load_b16(rwi, kr * (DF * 2) + 512, 0, 0);
        }
      }
      SCHED();
      u32x4 mf[4][2], mi[4][2];
      const int pq = c & 1;
#pragma unroll
      for (int t = 0; t < 4; ++t) {
        const int phi = 8 * t + (c >> 1);
        int k = p + 64 * phi; float ph0 = -0.25f * (float)pq, ph1 = ph0 + 0.25f, m0 = 1.f, m1 = 1.f;
        if (p == 0) {
          ph1 = ph0;
          if (t < 2) m1 = 0.f; else { m0 = 0.f; k -= 992; }
          if (t == 0 && c == 1) { k = 1024; ph0 = 0.f; }
        }
        const float kf = (float)k * (1.f / 2048.f), a = (float)g * kf, b = a + 4.f * kf, K = 2.f * cosrev(4.f * kf);
        float u0 = smul(cosrev(sadd(a, ph0)), m0), v0 = smul(cosrev(sadd(a, ph1)), m1), u1 = smul(cosrev(sadd(b, ph0)), m0), v1 = smul(cosrev(sadd(b, ph1)), m1);
        mf[t][0][0] = pk_f16(u0, v0); mf[t][0][1] = pk_f16(u1, v1);
#pragma unroll
        for (int n = 2; n < 8; ++n) {
          const float u2 = sfma(K, u1, -u0), v2 = sfma(K, v1, -v0); u0 = u1; u1 = u2; v0 = v1; v1 = v2;
          mf[t][n >> 2][n & 3] = pk_f16(u2, v2);
        }
      }
#pragma unroll
      for (int t = 0; t < 4; ++t) { pin(mf[t][0]); pin(mf[t][1]); }
      SCHED();
      if (q == 0) {
        const u64 t = (((u64)ep[1] << 32) | ep[0]) + 1;
        if (i == 0) {
          const u64 b0 = t << 8;
          __builtin_amdgcn_raw_buffer_store_b64(u32x2{(u32)b0, (u32)(b0 >> 32)}, rws, lane < 2 ? WS_CV + (j * 2 + lane) * 256 : OOB, 0, SC1);
          WAITV(0);
        }
        *(LAS u32x2*)(L + LDS_TAG + lane * 8) = u32x2{(u32)t, (u32)(t >> 32)};
        prog_set(L, w, lane, 1);
      }
      SCHED();
      {
        const int which = c & 1, kq = (c >> 1) & 3;
        const u32 msk0 = (p == 0 && which) ? 0u : ~0u, msk1 = (p == 0 && !which) ? 0u : ~0u;
        const u32 sgn = (p != 0 && (c & 2)) ? 0x80008000u : 0u;
        const u32 rsh = (kq & 1) * 16, rsg = kq == 0 ? 0u : kq == 1 ? 0x00008000u : kq == 2 ? 0x80008000u : 0x80000000u;
#pragma unroll
        for (int nu = 0; nu < 4; ++nu) {
          const float s2 = (float)(8 * nu + (c >> 1));
          const float beta = p != 0 ? sfma(s2, (float)p * (1.f / 2048.f), 0.25f * (float)which) : (which ? s2 * (1.f / 64.f) : 0.f);
#pragma unroll
          for (int r = 0; r < 2; ++r) {
            const float a = sfma(s2, (float)(2 * g + r) * (1.f / 32.f), beta);
            u32 v = pk_bf16(smul(cosrev(a), 0x1p-14f), smul(cosrev(sadd(a, -0.25f)), 0x1p-14f));
            const u32 v2 = __builtin_amdgcn_alignbit(v, v, rsh) ^ rsg;
            if (r == 0 && p == 0 && g == 0 && !which) v = (v & 0xffffu) | ((c & 2) ? 0xb8800000u : 0x38800000u);
            mi[nu][0][r] = v & msk0; mi[nu][1][r] = (v ^ sgn) & msk1;
            mi[nu][0][r + 2] = v2 & msk0; mi[nu][1][r + 2] = (v2 ^ sgn) & msk1;
          }
        }
      }
#pragma unroll
      for (int t = 0; t < 4; ++t) { pin(mi[t][0]); pin(mi[t][1]); }
      SCHED();
      STAMP(1);
      float ca[8], cb[8], cc[8], cd[8], ca2[8], cc2[8], aO, bO, aO2;
#pragma unroll
      for (int e = 0; e < 8; ++e) {
        const float rp = bf2f(raw[e][0]), ip = bf2f(raw[e][1]), rm = bf2f(raw[e][2]), im = bf2f(raw[e][3]);
        ca[e] = sadd(rp, rm); cb[e] = sadd(ip, im); cc[e] = sadd(im, -ip); cd[e] = sadd(rp, -rm);
        if (e == 0) {
          aO = ca[0]; bO = cb[0];
          if (p == 0 && g == 0) { ca[0] = rp; cb[0] = ip; cc[0] = 0.f; cd[0] = 0.f; aO = rm; bO = im; }
          aO2 = aO;
        }
        ca2[e] = ca[e]; cc2[e] = cc[e];
      }
      if (spec) {
        const bool sl = c == 0;
        const float hf = sl ? 0.5f : 1.f, zf = sl ? 0.f : 1.f;
        const float r0 = smul(bf2f(nyr), 0.5f), i0 = smul(bf2f(nyi), 0.5f);
        const float r1 = asf(__builtin_amdgcn_mov_dpp((int)asu(r0), 0xb1, 0xf, 0xf, true)), i1 = asf(__builtin_amdgcn_mov_dpp((int)asu(i0), 0xb1, 0xf, 0xf, true));
        float na = sadd(r0, r1), nc = sadd(i1, -i0), no = na;
        if (p == 0 && g == 0 && sl) { na = r0; nc = 0.f; no = r1; }
        sfor<0, 8>([&](auto E_) AINL {
          constexpr int e = decltype(E_)::value;
          const float va = e == 0 ? na : asf(__builtin_amdgcn_mov_dpp((int)asu(na), 0x100 + 2 * e, 0xf, 0xf, true));
          const float vc = e == 0 ? nc : asf(__builtin_amdgcn_mov_dpp((int)asu(nc), 0x100 + 2 * e, 0xf, 0xf, true));
          ca2[e] = sl ? va : ca2[e]; cc2[e] = sl ? vc : cc2[e];
          ca[e] = smul(ca[e], hf); cc[e] = smul(cc[e], hf); cb[e] = smul(cb[e], zf); cd[e] = smul(cd[e], zf);
        });
        aO2 = sl ? no : aO2; aO = smul(aO, hf); bO = smul(bO, zf);
      }
#pragma unroll
      for (int e = 0; e < 8; ++e) { pin(ca[e]); pin(cb[e]); pin(cc[e]); pin(cd[e]); pin(ca2[e]); pin(cc2[e]); }
      pin(aO); pin(bO); pin(aO2);
      const int aoff = WS_A + (((16 * q + (lane >> 4)) * 4 + (p >> 3)) * 16 + ((lane >> 3) & 1) * 8 + j) * 1024 +
                       ((2 * hq + ((lane >> 2) & 1)) * 16 + 2 * (p & 6) + (lane & 3)) * 16;
      prog_wait<1>(L, gP0, 1);
      tag = *(LAS u32x2*)(L + LDS_TAG + lane * 8);
      STAMP(2);
      __builtin_amdgcn_s_setprio(2);
      if (q == 0) {
        prog_wait<2>(L, gC, PC_W1 + TPOLL);
        STAMP(3);
        flag_wait<1, 4>(rws, WS_FT + (j * 4 + 2 * hq) * 32 * 128, lane, 64, tag);
        prog_set(L, w, lane, 2);
      } else prog_wait<1>(L, gP0, 2);
      STAMP(5);
      u32 tb[2][8];
#pragma unroll
      for (int n = 0; n < 8; ++n)
#pragma unroll
        for (int tl = 0; tl < 2; ++tl)
          tb[tl][n] = __builtin_amdgcn_raw_buffer_load_b32(rws, WS_T + ((j * 32 + p) * 32 + g) * 256 + hq * 128 + tl * 64 + c * 4, n * 1024, SC1);
      SCHED();
      f32x4 pqv[2][4];
#pragma unroll
      for (int t = 0; t < 4; ++t)
#pragma unroll
        for (int tl = 0; tl < 2; ++tl) {
          const u32x4 b0 = {tb[tl][0], tb[tl][1], tb[tl][2], tb[tl][3]}, b1 = {tb[tl][4], tb[tl][5], tb[tl][6], tb[tl][7]};
          pqv[tl][t] = mfma_h(mf[t][0], b0, f32x4{0.f, 0.f, 0.f, 0.f}); pqv[tl][t] = mfma_h(mf[t][1], b1, pqv[tl][t]);
        }
      STAMP(6);
      u32x4 eo[2][2];
#pragma unroll
      for (int e = 0; e < 8; ++e) {
        const float Pr = pqv[0][e >> 1][2 * (e & 1)], Qr = pqv[0][e >> 1][2 * (e & 1) + 1], Pi = pqv[1][e >> 1][2 * (e & 1)], Qi = pqv[1][e >> 1][2 * (e & 1) + 1];
        const float a_o = e == 0 ? aO : ca[e], b_o = e == 0 ? bO : cb[e], a_o2 = e == 0 ? aO2 : ca2[e];
        float Er = smul(Pr, ca[e]), Ei = smul(Pi, ca2[e]), Or = smul(Pr, cc[e]), Oi = smul(Pi, cc2[e]);
        Er = sfma(Pi, -cb[e], Er); Ei = sfma(Pr, cb[e], Ei); Or = sfma(Pi, -cd[e], Or); Oi = sfma(Pr, cd[e], Oi);
        Er = sfma(Qr, -cc[e], Er); Ei = sfma(Qi, -cc2[e], Ei); Or = sfma(Qr, a_o, Or); Oi = sfma(Qi, a_o2, Oi);
        Er = sfma(Qi, cd[e], Er); Ei = sfma(Qr, -cd[e], Ei); Or = sfma(Qi, -b_o, Or); Oi = sfma(Qr, b_o, Oi);
        eo[0][e >> 2][e & 3] = pk_bf16(Er, Or); eo[1][e >> 2][e & 3] = pk_bf16(Ei, Oi);
        SCHED();
        if (e == 3) {
#pragma unroll
          for (int tl = 0; tl < 2; ++tl)
#pragma unroll
            for (int nu = 0; nu < 2; ++nu) pqv[tl][nu] = mfma_bf(eo[tl][0], mi[nu][0], f32x4{0.f, 0.f, 0.f, 0.f});
          SCHED();
        }
      }
#pragma unroll
      for (int tl = 0; tl < 2; ++tl)
#pragma unroll
        for (int nu = 0; nu < 2; ++nu) pqv[tl][nu] = mfma_bf(eo[tl][1], mi[nu][1], pqv[tl][nu]);
#pragma unroll
      for (int tl = 0; tl < 2; ++tl)
#pragma unroll
        for (int nu = 2; nu < 4; ++nu) { pqv[tl][nu] = mfma_bf(eo[tl][0], mi[nu][0], f32x4{0.f, 0.f, 0.f, 0.f}); pqv[tl][nu] = mfma_bf(eo[tl][1], mi[nu][1], pqv[tl][nu]); }
      {
        const int sa = LDS_TT + (((c >> 1) * 4 + (g >> 1)) * 4 + 2 * q + (c & 1)) * 16 + (g & 1) * 8;
#pragma unroll
        for (int nu = 0; nu < 4; ++nu)
#pragma unroll
          for (int tl = 0; tl < 2; ++tl)
            *(LAS u32x2*)(L + sa + nu * 2048 + tl * 128) = u32x2{pk_bf16(pqv[tl][nu][0], pqv[tl][nu][1]), pk_bf16(pqv[tl][nu][2], pqv[tl][nu][3])};
      }
      prog_set(L, w, lane, 3);
      prog_wait(L, gP, 3);
#pragma unroll
      for (int n = 0; n < 4; ++n) {
        const u32x4 v = lds128(L, LDS_TT + ((q * 4 + n) * 64 + lane) * 16);
        st128(v, rws, aoff + n * 262144);
      }
      STAMP(7);
      WAITV(0);
      if (lane == 0) {
        const u64 old = __hip_atomic_fetch_add((GAS u64*)(ws + WS_CV + (j * 2 + q) * 256), 1ull, __ATOMIC_RELAXED, __HIP_MEMORY_SCOPE_AGENT);
        if ((old & 0xffull) == 31ull) __builtin_amdgcn_raw_buffer_store_b64(tag, rws, WS_DV + (q * 8 + j) * 128, 0, SC1);
      }
    }
    STAMP(8);
    prog_set(L, w, lane, 4);
    STAMP(9);
    if (q == 0) {
      flag_wait<1, 4>(rws, WS_DV + (i3 >> 4) * 8 * 128, lane, 8, tag);
      prog_set(L, w, lane, 5);
      if (BID == 0 && lane == 0) __builtin_amdgcn_raw_buffer_store_b64(tag, rws, WS_EPOCH, 0, SC1);
    }
    STAMP(11);
    {
      const u32 aoff = WS_A + (i3 * 4 + 2 * q) * 16384 + lane * 16;
      if (q == 1) prog_wait<1>(L, gP0, 5);
      constexpr int AW = 6;
      u32x4 a0[AW], a1[AW], fi[8];
      f32x4 pm[2][4];
#define A_LOAD(ks) do { a0[(ks) % AW] = __builtin_amdgcn_raw_buffer_load_b128(rws, aoff, (((ks) + rotA) & 15) * 1024, SC1); \
                        a1[(ks) % AW] = __builtin_amdgcn_raw_buffer_load_b128(rws, aoff + 16384, (((ks) + rotA) & 15) * 1024, SC1); } while (0)
#pragma unroll
      for (int ks = 0; ks < AW; ++ks) { A_LOAD(ks); SCHED(); }
      prog_wait<1>(L, gC, PC_FI); prog_wait<1>(L, gP, 4);
#pragma unroll
      for (int n = 0; n < 8; ++n) fi[n] = lds128(L, LDS_FI + n * 1024 + lane * 16);
      SCHED();
#pragma unroll
      for (int n = 0; n < 64; ++n) {
        const int ks = n >> 2;
        MFA(n < 4, pm[0][n & 3], a0[ks % AW], fi[n & 7]);
        SCHED();
        MFA(n < 4, pm[1][n & 3], a1[ks % AW], fi[n & 7]);
        if (n + 8 < 64) fi[n & 7] = lds128(L, LDS_FI + (n + 8) * 1024 + lane * 16);
        if ((n & 3) == 3 && ks + AW < 16) A_LOAD(ks + AW);
        SCHED();
      }
      MFMA_SETTLE();
#pragma unroll
      for (int tt = 0; tt < 2; ++tt)
#pragma unroll
        for (int nu = 0; nu < 4; ++nu)
          *(LAS u32x2*)(L + LDS_PM + (16 * nu + c) * PMROW + (16 * (2 * q + tt) + 4 * g) * 2) =
              u32x2{pk_bf16(pm[tt][nu][0], pm[tt][nu][1]), pk_bf16(pm[tt][nu][2], pm[tt][nu][3])};
      prog_set(L, w, lane, 6);
    }
    STAMP(12);
    STAMP_OUT();
    return;
  }

  u32x4 Ff[16];
  {
    int kd = 32 * j + 16 * (w >> 1) + c; float ph = (w & 1) ? 0.25f : 0.f;
    if (kd == 0 && (w & 1)) { kd = 256; ph = -0.25f; }
    const float step = (float)kd * (1.f / 512.f), K = 2.f * cosrev(2.f * step);
#pragma unroll
    for (int a = 0; a < 4; ++a) {
      const int k0 = 128 * g + 32 * ((a + w) & 3);
      gen32((float)(((2 * k0 + 1) * kd) & 1023) * (1.f / 1024.f) + ph, step, K, &Ff[4 * a]);
    }
  }
#pragma unroll
  for (int n = 0; n < 16; ++n) pin(Ff[n]);
  STAMP(1);
  const f32x4 z4 = {0.f, 0.f, 0.f, 0.f};
  const int gC = pga_of(0, 4, lane), gL = pga_of(4, 2, lane);
#define gP pga_of(6, 2, lane)
#define gP0 pga_of(6, 1, lane)
  f32x4 xa[4];
  u32x4 Gf[4][2];
  {
    f32x4 sacc, qacc[4];
    int abase[4];
#pragma unroll
    for (int a = 0; a < 4; ++a) { abase[a] = LDS_X + c * XROW + (16 * g + 4 * ((a + w) & 3)) * 16; pin(abase[a]); }
    const float gph = (c & 1) ? -0.25f : 0.f, gk0 = (float)(c >> 1) * (1.f / 64.f), gs0 = (float)(4 * g + 16 * rotx);
    const u32 gsg = (c & 2) ? 0x80008000u : 0u;
    int so_ = c < 4 ? LDS_ST + w * 256 + (16 * c + 4 * g) * 4 : LDS_DUMMY + lane * 16;
    int qo_ = (c >> 2) == g ? LDS_ST + 1024 + w * 256 + c * 4 : LDS_DUMMY + lane * 4;
    pin(so_); pin(qo_);
    u32x4 sel[4];
#pragma unroll
    for (int t = 0; t < 4; ++t) { const u32 sv = c == t ? 0x3f803f80u : 0u; sel[t] = u32x4{sv, sv, sv, sv}; pin(sel[t]); }
#define GGEN(id) do { const int gt = (id) >> 2, gr = (id) & 3; const float kf = gk0 + (float)gt * 0.125f; \
      const float ga = sfma(gs0 + (float)(16 * (gr >> 1) + 2 * (gr & 1)), kf, gph); \
      u32 v = pk_f16(cosrev(ga), cosrev(sadd(ga, kf))); if (gt == 0 && c == 1) v = 0xbc003c00u; \
      u32 v2 = v ^ gsg; pin(v); pin(v2); Gf[gt][0][gr] = v; Gf[gt][1][gr] = v2; } while (0)
    GGEN(0); GGEN(1); GGEN(2); GGEN(3); GGEN(4); GGEN(5);
    u32x4 af[16];
    prog_wait(L, gL, 1);
    STAMP(2);
    u32 pv = prog_get(L, gL);
#pragma unroll
    for (int n = 0; n < 16; ++n) af[n] = lds128(L, abase[n >> 2] + (n & 3) * 16);
    sfor<0, 4>([&](auto T_) AINL {
      constexpr int t = decltype(T_)::value;
      if (t == 0) { SCHED(); GGEN(6); GGEN(7); GGEN(8); GGEN(9); GGEN(10); }
      if (t < 3) { if (prog_bad(pv, t + 2)) prog_spin(L, gL, t + 2); }
      SCHED();
#pragma unroll
      for (int n = 0; n < 16; ++n) {
        if (n < 4) { MFA(t == 0 && n == 0, sacc, af[n], sel[t]); MFA(n == 0, qacc[t], af[n], af[n]); }
        MFA(n == 0, xa[t], af[n], Ff[n]);
        if (t < 3) af[n] = lds128(L, abase[n >> 2] + (n & 3) * 16 + (t + 1) * 16 * XROW);
        if (t < 2 && n == 3) pv = prog_get(L, gL);
        if (n == 2 && t > 0) {
          const float lo = (c & 1) ? qacc[(t + 3) & 3][1] : qacc[(t + 3) & 3][0], hi = (c & 1) ? qacc[(t + 3) & 3][3] : qacc[(t + 3) & 3][2];
          *(LAS float*)(L + qo_ + (t - 1) * 64) = (c & 2) ? hi : lo;
        }
        if (t == 3 && n == 7) {
          *(LAS f32x4*)(L + so_) = sacc;
          const float lo = (c & 1) ? qacc[3][1] : qacc[3][0], hi = (c & 1) ? qacc[3][3] : qacc[3][2];
          *(LAS float*)(L + qo_ + 3 * 64) = (c & 2) ? hi : lo;
          prog_set(L, w, lane, PC_STAT);
        }
        if (t == 3 && n >= 8 && n < 13) GGEN(n + 3);
        SCHED();
      }
    });
  }
  STAMP(3);
  prog_wait(L, gC, PC_STAT);
  {
    float sm = 0.f, sq = 0.f;
#pragma unroll
    for (int a = 0; a < 4; ++a) { sm += *(LAS float*)(L + LDS_ST + a * 256 + lane * 4); sq += *(LAS float*)(L + LDS_ST + 1024 + a * 256 + lane * 4); }
    const float mean = sm * (1.f / 512.f);
    const float var = fmaxf(sq * (1.f / 512.f) - mean * mean, 0.f);
    const float sd = rbf(__builtin_amdgcn_sqrtf(rbf(rbf(var) + 1e-5f)));
    *(LAS float*)(L + LDS_ST2 + w * 512 + lane * 4) = rbf(mean);
    *(LAS float*)(L + LDS_ST2 + w * 512 + 256 + lane * 4) = __builtin_amdgcn_rcpf(sd);
    FENCE();
  }
  {
    u32x4 xb[2];
    f32x4 rv[4];
#pragma unroll
    for (int t = 0; t < 4; ++t) rv[t] = *(LAS f32x4*)(L + LDS_ST2 + w * 512 + 256 + (16 * t + 4 * g) * 4);
    if (j == 0 && w == 0) {
      const float mco = c == 0 ? 512.f : 0.f;
#pragma unroll
      for (int t = 0; t < 4; ++t) xa[t] -= mco * *(LAS f32x4*)(L + LDS_ST2 + w * 512 + (16 * t + 4 * g) * 4);
    }
#pragma unroll
    for (int t = 0; t < 4; ++t) {
      const f32x4 v = xa[t] * (rv[t] * 0.03125f);
      xb[t >> 1][2 * (t & 1)] = pk_f16(v[0], v[1]);
      xb[t >> 1][2 * (t & 1) + 1] = pk_f16(v[2], v[3]);
    }
    f32x4 tt[4];
#pragma unroll
    for (int t = 0; t < 4; ++t) tt[t] = mfma_h(Gf[t][0], xb[0], z4);
#pragma unroll
    for (int t = 0; t < 4; ++t) tt[t] = mfma_h(Gf[t][1], xb[1], tt[t]);
#pragma unroll
    for (int t = 0; t < 4; ++t)
#pragma unroll
      for (int sub = 0; sub < 2; ++sub)
        *(LAS u32*)(L + LDS_TT + w * 2048 + (8 * t + 2 * g + sub) * 64 + c * 4) = pk_f16(tt[t][2 * sub], tt[t][2 * sub + 1]);
  }
  FENCE();
  const u32x4 tv0 = lds128(L, LDS_TT + w * 2048 + lane * 16), tv1 = lds128(L, LDS_TT + w * 2048 + 1024 + lane * 16);
  const __amdgpu_buffer_rsrc_t rws = MK_RWS();
  const u32 toff = WS_T + ((j * 32 + (lane >> 2)) * 32 + i) * 256 + w * 64 + (lane & 3) * 16;
  u32x2 tag;
  STAMP(4);
  f32x4 acc[3][5];
  {
    const int xbase = LDS_X + c * XROW + 16 * g * 16;
    int wbA[2], wbB[2];
    wbA[0] = LDS_RING + (4 * g + (c >> 2)) * 352 + (3 * w) * 32 + (c & 3) * 8; wbB[0] = wbA[0] + (w == 3 ? 32 : 64);
    wbA[1] = wbA[0] + 4 * W1CH; wbB[1] = wbB[0] + 4 * W1CH;
    pin(wbA[0]); pin(wbA[1]); pin(wbB[0]); pin(wbB[1]);
    u32x4 xf[2][5], wf[2][3];
#define TRH(dst, h, addr) do { const u32x2 t_ = ldstr(L, addr); dst[2 * (h)] = t_[0]; dst[2 * (h) + 1] = t_[1]; } while (0)
    xf[0][4] = u32x4{0x3f803f80u, 0x3f803f80u, 0x3f803f80u, 0x3f803f80u}; pin(xf[0][4]); xf[1][4] = xf[0][4];
#define RD1(n, b, r) do { const int sl_ = ((n) + 5) % NSLOT, so_ = (sl_ & 3) * W1CH, wbase = wbA[sl_ >> 2], wbase2 = wbB[sl_ >> 2]; \
      if ((r) < 2) TRH(wf[b][0], (r) & 1, wbase + so_ + ((r) & 1) * 16 * 352); \
      else if ((r) < 6) xf[b][((r) - 2) & 3] = lds128(L, xo + (((r) - 2) & 3) * 16 * XROW); \
      else if ((r) < 8) TRH(wf[b][1], (r) & 1, wbase + so_ + 32 + ((r) & 1) * 16 * 352); \
      else TRH(wf[b][2], (r) & 1, wbase2 + so_ + ((r) & 1) * 16 * 352); } while (0)
    prog_wait(L, gP0, 1);
    prog_wait(L, gL, PL_W1 + 1);
    {
      const int xo = xbase + rot1 * 16;
#pragma unroll
      for (int r = 0; r < 10; ++r) RD1(0, 0, r);
    }
    prog_set(L, w, lane, PC_W1);
    bool bad = false;
#pragma unroll
    for (int n = 0; n < 16; ++n) {
      if (__builtin_expect(bad, 0)) prog_spin(L, gL, PL_W1 + n + 1);
      const int xo = xbase + ((n + 1 + rot1) & 15) * 16;
      u32 pv = 0;
      SCHED();
#pragma unroll
      for (int q = 0; q < 15; ++q) {
        const int u = q / 5, t = q % 5;
        MFA(n == 0, acc[u][t], wf[n & 1][u], xf[n & 1][t]);
        if (n + 1 < 16) {
          if (q < 10) RD1(n + 1, (n + 1) & 1, q);
          if (q == 10) prog_set(L, w, lane, PC_W1 + n + 1);
          if (q == 7 && n + 2 < 16) pv = prog_get(L, gL);
          if (q == 13 && n + 2 < 16) bad = prog_bad(pv, PL_W1 + n + 2);
          if (n == 0 && q == 1) st128(tv0, rws, toff);
          if (n == 0 && q == 3) st128(tv1, rws, toff + 16 * 8192);
          if (n == TFSTEP - 1 && q == 11) tag = *(LAS u32x2*)(L + LDS_TAG + lane * 8);
          if (n == TFSTEP && q == 3) { WAITV(0); st_flag(tag, rws, WS_FT + ((j * 4 + w) * 32 + i) * 128, lane); }
        }
        SCHED();
      }
      if (n + 2 >= 16) bad = false;
    }
    MFMA_SETTLE();
  }
  STAMP(5);
  {
    const int hlo = WS_H + (i * 4 + (((g & 1) + rotx) & 3)) * 43 * 1024 + ((g >> 1) * 16 + c) * 16;
    const int hhi = WS_H + (i * 4 + (((g & 1) + 2 + rotx) & 3)) * 43 * 1024 + ((g >> 1) * 16 + c) * 16;
    float mrow[4], rrow[4], r2row[4];
#pragma unroll
    for (int t = 0; t < 4; ++t) {
      mrow[t] = -*(LAS float*)(L + LDS_ST2 + w * 512 + (16 * t + c) * 4);
      rrow[t] = *(LAS float*)(L + LDS_ST2 + w * 512 + 256 + (16 * t + c) * 4);
      r2row[t] = rrow[t] * -1.4426950408889634f;
    }
#pragma unroll
    for (int u = 0; u < 3; ++u) {
      if (3 * w + u < nt) {
        u32 hp[4][2];
        {
          f32x2 tv[8], ar[8];
#pragma unroll
          for (int e = 0; e < 8; ++e) {
            const int t = e >> 1, hh = e & 1;
            tv[e] = f32x2{acc[u][4][2 * hh], acc[u][4][2 * hh + 1]} * mrow[t] + f32x2{acc[u][t][2 * hh], acc[u][t][2 * hh + 1]};
          }
          SCHED();
#pragma unroll
          for (int e = 0; e < 8; ++e) ar[e] = tv[e] * r2row[e >> 1];
          SCHED();
#pragma unroll
          for (int e = 0; e < 8; ++e) { ar[e] = f32x2{__builtin_amdgcn_exp2f(ar[e][0]), __builtin_amdgcn_exp2f(ar[e][1])}; tv[e] *= rrow[e >> 1]; }
          SCHED();
#pragma unroll
          for (int e = 0; e < 8; ++e) ar[e] += 1.f;
          SCHED();
#pragma unroll
          for (int e = 0; e < 8; ++e) ar[e] = f32x2{__builtin_amdgcn_rcpf(ar[e][0]), __builtin_amdgcn_rcpf(ar[e][1])};
          SCHED();
#pragma unroll
          for (int e = 0; e < 8; ++e) tv[e] *= ar[e];
          SCHED();
#pragma unroll
          for (int e = 0; e < 8; ++e) hp[e >> 1][e & 1] = pk_bf16v(tv[e]);
        }
#pragma unroll
        for (int tp = 0; tp < 2; ++tp) {
          auto a = __builtin_amdgcn_permlane16_swap(hp[2 * tp][0], hp[2 * tp + 1][0], false, false);
          auto b = __builtin_amdgcn_permlane16_swap(hp[2 * tp][1], hp[2 * tp + 1][1], false, false);
          const u32x4 o = {a[0], b[0], a[1], b[1]};
          st128(o, rws, (tp ? hhi : hlo) + (u0 + 3 * w + u) * 512);
        }
      }
    }
  }
  STAMPX(6);
  const int orow = 32 * (16 * w + c) + i3;
  {
    const float st = (float)(2 * (64 * j3 + 16 * w + c) + 1) * (1.f / 1024.f);
    const float K = 2.f * cosrev(32.f * st);
    int fa = LDS_FI + w * 1024 + lane * 16; pin(fa);
#pragma unroll
    for (int reim = 0; reim < 2; ++reim) {
      u32x4 fo[8];
#pragma unroll
      for (int r = 0; r < 4; ++r) {
        const float a0 = (float)(8 * g + 2 * r) * st + (reim ? 0.25f : 0.f), a1 = a0 + 32.f * st;
        f32x2 pa = {cosrev(a0), cosrev(a0 + st)}, pb = {cosrev(a1), cosrev(a1 + st)};
        f32x2 p0 = pa;
        if (reim == 1 && r == 0 && g == 0) p0[0] = (c & 1) ? -1.f : 1.f;
        fo[0][r] = pk_bf16v(p0); fo[1][r] = pk_bf16v(pb);
#pragma unroll
        for (int jk = 2; jk < 8; ++jk) { const f32x2 pc = K * pb - pa; pa = pb; pb = pc; fo[jk][r] = pk_bf16v(pc); }
      }
#pragma unroll
      for (int jk = 0; jk < 8; ++jk) pin(fo[jk]);
      if (reim == 0) prog_wait(L, gC, PC_W1 + 15);
#pragma unroll
      for (int jk = 0; jk < 8; ++jk) *(LAS u32x4*)(L + fa + ((reim * 8 + jk - rotA) & 15) * 4096) = fo[jk];
      if (reim == 0) { WAITV(0); st_flag(tag, rws, WS_FH + ((i * 8 + j) * 4 + w) * 128, lane); }
    }
  }
  prog_set(L, w, lane, PC_FI);
  STAMPX(7);
  u32x4 gi[2];
  {
    const float sf = (float)(16 * w + c) * (1.f / 64.f);
#pragma unroll
    for (int kp = 0; kp < 2; ++kp)
#pragma unroll
      for (int r = 0; r < 4; ++r) {
        const float a = (float)(16 * kp + 4 * g + r) * sf;
        float hi = cosrev(a - 0.25f);
        if (kp == 0 && r == 0 && g == 0) hi = (c & 1) ? -1.f : 1.f;
        gi[kp][r] = pk_bf16(cosrev(a), hi);
      }
    pin(gi[0]); pin(gi[1]);
  }
  u32x2 xv[4];
#pragma unroll
  for (int nu = 0; nu < 4; ++nu) xv[nu] = *(const GAS u32x2*)(x + orow * D_ + 64 * j3 + 16 * nu + 4 * g);
  f32x4 oa[4], ob[4];
  {
    const int kh = w & 1;
    constexpr int HW = 4;
    u32x4 ha[HW], hb[HW];
    const int hoa = WS_H + (i3 * 4 + w) * 43 * 1024 + lane * 16, hob = WS_H + (i3 * 4 + (w ^ 1)) * 43 * 1024 + lane * 16;
    u32 hso = (kh + rot2) * 1024; if (hso >= 43 * 1024) hso -= 43 * 1024;
#define HNEXT() do { hso += 2048; if (hso >= 43 * 1024) hso -= 43 * 1024; } while (0)
    int wb1[4], wb2[4];
    constexpr int WB1 = LDS_RING, WB2 = LDS_RING + 7 * 8192;
    {
      const int row = 8 * g + (c >> 2), swz = ((row >> 1) & 1) | (((row >> 3) & 1) << 1);
#pragma unroll
      for (int nu = 0; nu < 4; ++nu) {
        wb1[nu] = WB1 + kh * 4096 + row * 128 + ((nu ^ swz) * 32) + (c & 3) * 8; wb2[nu] = wb1[nu] + (WB2 - WB1);
        pin(wb1[nu]); pin(wb2[nu]);
      }
    }
#define W2ADDR(nu, un) (u2slot(un) >= WB2 ? wb2[nu] + (u2slot(un) - WB2) : wb1[nu] + (u2slot(un) - WB1))
    u32x4 wf[U2N][4];
#define W2RD(k) TRH(wf[((k) >> 3) % U2N][((k) & 7) >> 1], (k) & 1, W2ADDR(((k) & 7) >> 1, (k) >> 3) + ((k) & 1) * 512)
    prog_wait(L, gL, PL_W2 + U2N - 1);
    STAMPX(8);
#pragma unroll
    for (int k = 0; k < 8 * U2N; ++k) { W2RD(k); if ((k & 7) == 7) prog_set(L, w, lane, pcw2(k >> 3)); }
#pragma unroll
    for (int n = 0; n < U2N; ++n)
#pragma unroll
      for (int r = 0; r < 4; ++r) pin(wf[n][r]);
    STAMPX(9);
    if (w == 0) {
      flag_wait<1, 2>(rws, WS_FH + i3 * 32 * 128, lane, 32, tag);
      prog_set(L, w, lane, PC_HS);
    } else prog_wait(L, pga_of(0, 1, lane), PC_HS);
    STAMPX(10);
#pragma unroll
    for (int n = 0; n < HW; ++n) {
      ha[n] = __builtin_amdgcn_raw_buffer_load_b128(rws, hoa, hso, SC1); SCHED();
      hb[n] = __builtin_amdgcn_raw_buffer_load_b128(rws, hob, hso, SC1);
      HNEXT(); SCHED();
    }
    prog_wait(L, gL, PL_W2 + 2 * U2N - 1);
    bool bad = false;
#pragma unroll
    for (int un = 0; un < 21; ++un) {
      const int k0 = 8 * U2N + 6 * (un - 1), kn1 = 8 * U2N + 6 * (un + 1) - 1 < 175 ? 8 * U2N + 6 * (un + 1) - 1 : 175;
      const int need = (k0 + 5 < 175 ? k0 + 5 : 175) >> 3;
      if (__builtin_expect(bad, 0)) prog_spin(L, gL, PL_W2 + need);
      bad = false;
      const bool chk = (kn1 >> 3) >= 2 * U2N && k0 + 6 < 176;
      u32 pv = 0;
      SCHED();
#pragma unroll
      for (int q = 0; q < 8; ++q) {
        const int nu = q >> 1;
        const u32x4 wa = wf[un % U2N][nu];
        if (q & 1) MFA(un == 0, ob[nu], wa, hb[un % HW]); else MFA(un == 0, oa[nu], wa, ha[un % HW]);
        if (q == 0 && chk) pv = prog_get(L, gL);
        if (un >= 1 && q < 6 && k0 + q < 176) {
          W2RD(k0 + q);
          if (((k0 + q) & 7) == 7 && ((k0 + q) >> 3) + U2N < 22) prog_set(L, w, lane, pcw2((k0 + q) >> 3));
        }
        if (q == 5 && chk) bad = prog_bad(pv, PL_W2 + (kn1 >> 3));
        if (un + HW < 22) {
          if (q == 6) ha[un % HW] = __builtin_amdgcn_raw_buffer_load_b128(rws, hoa, hso, SC1);
          if (q == 7) { hb[un % HW] = __builtin_amdgcn_raw_buffer_load_b128(rws, hob, hso, SC1); HNEXT(); }
        }
        SCHED();
      }
    }
    if (kh == 0) {
#pragma unroll
      for (int nu = 0; nu < 4; ++nu) {
        const u32x4 wa = wf[21 % U2N][nu];
        mfa(oa[nu], wa, ha[21 % HW]); mfa(ob[nu], wa, hb[21 % HW]);
      }
    }
    MFMA_SETTLE();
  }
  STAMPX(11);
  prog_wait(L, gP, 6);
#pragma unroll
  for (int nu = 0; nu < 4; ++nu) *(LAS f32x4*)(L + LDS_RED + ((w ^ 1) * 4 + nu) * 1024 + lane * 16) = ob[nu];
  prog_set(L, w, lane, PC_RED);
  {
    u32x4 pf[8];
#pragma unroll
    for (int n = 0; n < 8; ++n) pf[n] = lds128(L, LDS_PM + (16 * (n >> 1) + c) * PMROW + (32 * (n & 1) + 8 * g) * 2);
#pragma unroll
    for (int nu = 0; nu < 4; ++nu) oa[nu] = mfma_bf(pf[2 * nu], gi[0], oa[nu]);
#pragma unroll
    for (int nu = 0; nu < 4; ++nu) oa[nu] = mfma_bf(pf[2 * nu + 1], gi[1], oa[nu]);
  }
  prog_wait(L, pga_of(w ^ 1, 1, lane), PC_RED);
#pragma unroll
  for (int nu = 0; nu < 4; ++nu) {
    const int col = 64 * j3 + 16 * nu + 4 * g;
    const f32x4 v = oa[nu] + *(LAS f32x4*)(L + LDS_RED + (w * 4 + nu) * 1024 + lane * 16);
    const u32x2 o = {pk_bf16(v[0] + bf2f(xv[nu][0] & 0xffffu), v[1] + asf(xv[nu][0] & 0xffff0000u)),
                     pk_bf16(v[2] + bf2f(xv[nu][1] & 0xffffu), v[3] + asf(xv[nu][1] & 0xffff0000u))};
    *(GAS u32x2*)(out + orow * D_ + col) = o;
  }
  STAMPX(12);
  STAMPX(13);
  STAMP_OUT();
}

#undef x
#undef wr
#undef wi
#undef w1
#undef w2
#undef ws
#undef out
extern "C" void kernel_launch(void* const* d_in, const int* in_sizes, int n_in, void* d_out, int out_size, void* d_ws, size_t ws_size,
                              hipStream_t stream) {
  Args a;
  a.x = (const u16*)d_in[0]; a.wr = (const u16*)d_in[1]; a.wi = (const u16*)d_in[2];
  a.w1 = (const u16*)d_in[4]; a.w2 = (const u16*)d_in[5]; a.ws = (unsigned char*)d_ws; a.out = (u16*)d_out;
  (void)hipFuncSetAttribute((const void*)k_all, hipFuncAttributeMaxDynamicSharedMemorySize, LDS_BYTES);
  k_all<<<256, 512, LDS_BYTES, stream>>>(a);
}
```

```cpp
#include <hip/hip_runtime.h>
#include <hip/hip_bf16.h>

typedef unsigned u32;
typedef unsigned short u16;
typedef unsigned long long u64;
typedef u32 u32x4 __attribute__((ext_vector_type(4)));
typedef u32 u32x2 __attribute__((ext_vector_type(2)));
typedef float f32x4 __attribute__((ext_vector_type(4)));
typedef float f32x2 __attribute__((ext_vector_type(2)));
typedef __bf16 bf16x8 __attribute__((ext_vector_type(8)));
typedef __bf16 bf16x2 __attribute__((ext_vector_type(2)));
typedef _Float16 f16x8 __attribute__((ext_vector_type(8)));
typedef _Float16 f16x2 __attribute__((ext_vector_type(2)));
typedef short s16x4 __attribute__((ext_vector_type(4)));
#define LAS __attribute__((address_space(3)))
#define GAS __attribute__((address_space(1)))
#define DEVI __device__ __forceinline__

constexpr int S_ = 2048, D_ = 512, F_ = 1376, DF = 257;
constexpr u32 WS_EPOCH = 0;
constexpr u32 WS_CV = 65536;
constexpr u32 WS_DV = 65536 + 8192;
constexpr u32 WS_FT = 131072;
constexpr u32 WS_FH = 262144;
constexpr u32 WS_T = 393216;
constexpr u32 WS_H = WS_T + (2u << 20);
constexpr u32 WS_A = WS_H + 32u * 4 * 43 * 1024;
constexpr u32 WS_END = WS_A + (2u << 20);
constexpr int XROW = 1040;
constexpr int LDS_X = 0;
constexpr int W1CH = 11264;
constexpr int NSLOT = 7;
constexpr int LDS_RING = 64 * XROW;
constexpr int LDS_TT = LDS_RING + NSLOT * W1CH;
constexpr int LDS_ST = LDS_TT + 8192;
constexpr int LDS_ST2 = LDS_ST + 2048;
constexpr int LDS_PROG = LDS_ST2 + 2048;
constexpr int LDS_TAG = LDS_PROG + 2048;
constexpr int LDS_DUMMY = LDS_TAG + 512;
constexpr int LDS_BYTES = 163840;
constexpr int LDS_FI = 0;
constexpr int U2N = 9;
constexpr int LDS_PM = LDS_TT, PMROW = 144;
constexpr int LDS_RED = 0;
constexpr int LDS_PROG2 = LDS_DUMMY + 1024;
DEVI constexpr int u2slot(int n) { return LDS_RING + (n % U2N) * 8192; }
constexpr int PC_STAT = 1, PC_W1 = 3  , PC_FI = 21, PC_HS = 31, PC_RED = 50;
DEVI constexpr int pcw2(int n) { return n < 9 ? 22 + n : 23 + n; }
constexpr int PL_W1 = 5, PL_W2 = 21;
constexpr int SPIN_CAP = 1 << 15;
constexpr u32 SENT = 0xffffffffu;

DEVI float asf(u32 v) { return __builtin_bit_cast(float, v); }
DEVI u32 asu(float v) { return __builtin_bit_cast(u32, v); }
DEVI float cosrev(float x) { return __builtin_amdgcn_cosf(x); }
DEVI u32 pk_bf16(float a, float b) { f32x2 v = {a, b}; return __builtin_bit_cast(u32, __builtin_convertvector(v, bf16x2)); }
DEVI u32 pk_f16(float a, float b) { f32x2 v = {a, b}; return __builtin_bit_cast(u32, __builtin_convertvector(v, f16x2)); }
DEVI u32 pk_bf16v(f32x2 v) { return __builtin_bit_cast(u32, __builtin_convertvector(v, bf16x2)); }
DEVI u32 pk_f16v(f32x2 v) { return __builtin_bit_cast(u32, __builtin_convertvector(v, f16x2)); }
DEVI float rbf(float a) { return asf(pk_bf16(a, a) << 16); }
DEVI float bf2f(u32 h) { return asf(h << 16); }
DEVI f32x4 mfma_bf(u32x4 a, u32x4 b, f32x4 c) {
  return __builtin_amdgcn_mfma_f32_16x16x32_bf16(__builtin_bit_cast(bf16x8, a), __builtin_bit_cast(bf16x8, b), c, 0, 0, 0);
}
DEVI f32x4 mfma_h(u32x4 a, u32x4 b, f32x4 c) {
  return __builtin_amdgcn_mfma_f32_16x16x32_f16(__builtin_bit_cast(f16x8, a), __builtin_bit_cast(f16x8, b), c, 0, 0, 0);
}
DEVI void mfa(f32x4& c, u32x4 a, u32x4 b) { asm volatile("v_mfma_f32_16x16x32_bf16 %0, %1, %2, %0" : "+v"(c) : "v"(a), "v"(b)); }
DEVI void mfz(f32x4& c, u32x4 a, u32x4 b) { asm volatile("v_mfma_f32_16x16x32_bf16 %0, %1, %2, 0" : "=&v"(c) : "v"(a), "v"(b)); }
#define MFA(first, c, a, b) do { if (first) mfz(c, a, b); else mfa(c, a, b); } while (0)
#define MFMA_SETTLE() asm volatile("s_nop 7\n\ts_nop 1" ::: "memory")
DEVI float dot2bf(u32 a, u32 b, float c) {
  return __builtin_amdgcn_fdot2_f32_bf16(__builtin_bit_cast(bf16x2, a), __builtin_bit_cast(bf16x2, b), c, false);
}
DEVI u32x4 mk_srd(const void* p, u32 bytes) {
  u64 a = (u64)p;
  u32x4 r = {(u32)a, (u32)(a >> 32) & 0xffffu, bytes, 0x00020000u};
  return r;
}
DEVI void dma16(u32x4 srd, u32 voff, u32 soff, u32 lds) {
  asm volatile("s_mov_b32 m0, %3\n\ts_nop 0\n\tbuffer_load_dwordx4 %0, %1, %2 offen lds"
               :: "v"(voff), "s"(srd), "s"(soff), "s"(lds) : "memory");
}
DEVI void dma_x8(u32x4 srd, u32 voff, u32 soff, u32 lds) {
  u32 t;
#define DX_ "s_add_u32 m0, m0, 0x410\n\ts_add_u32 %0, %0, 0x8000\n\tbuffer_load_dwordx4 %1, %2, %0 offen lds\n\t"
  asm volatile("s_mov_b32 m0, %4\n\ts_mov_b32 %0, %3\n\tbuffer_load_dwordx4 %1, %2, %0 offen lds\n\t" DX_ DX_ DX_ DX_ DX_ DX_ DX_
               : "=&s"(t) : "v"(voff), "s"(srd), "s"(soff), "s"(lds) : "memory", "scc");
#undef DX_
}
DEVI void dma_4k(u32x4 srd, u32 v0, u32 v1, u32 v2, u32 v3, u32 soff, u32 lds) {
  asm volatile("s_mov_b32 m0, %6\n\ts_nop 0\n\tbuffer_load_dwordx4 %0, %4, %5 offen lds\n\tbuffer_load_dwordx4 %1, %4, %5 offen offset:1024 lds\n\t"
               "buffer_load_dwordx4 %2, %4, %5 offen offset:2048 lds\n\tbuffer_load_dwordx4 %3, %4, %5 offen offset:3072 lds"
               :: "v"(v0), "v"(v1), "v"(v2), "v"(v3), "s"(srd), "s"(soff), "s"(lds) : "memory");
}
DEVI void dma_w1tail(u32x4 srd, u32 v4, u32 v5, u32 soff) {
  asm volatile("s_add_u32 m0, m0, 0x1000\n\ts_nop 0\n\tbuffer_load_dwordx4 %0, %2, %3 offen lds\n\ts_mov_b32 exec_hi, 0\n\t"
               "buffer_load_dwordx4 %1, %2, %3 offen offset:1024 lds\n\ts_mov_b32 exec_hi, -1"
               :: "v"(v4), "v"(v5), "s"(srd), "s"(soff) : "memory", "scc");
}
DEVI void touch(u32x4 srd, u32 voff, u32 lds) {
  asm volatile("s_mov_b32 m0, %2\n\ts_nop 0\n\tbuffer_load_dword %0, %1, 0 offen lds" :: "v"(voff), "s"(srd), "s"(lds) : "memory");
}
#define WAITV_(n) asm volatile("s_waitcnt vmcnt(" #n ")" ::: "memory")
#define WAITV(n) WAITV_(n)
#define W2WIN 16
#define WAITL() asm volatile("s_waitcnt lgkmcnt(0)" ::: "memory")
#define FENCE() asm volatile("" ::: "memory")
#define SCHED() __builtin_amdgcn_sched_barrier(0)
#define BAR0() do { asm volatile("s_waitcnt lgkmcnt(0)\n\ts_barrier" ::: "memory"); SCHED(); } while (0)
#define NOUNROLL _Pragma("unroll 1")
DEVI u32x4 lds128(LAS unsigned char* L, int off) { return *(LAS u32x4*)(L + off); }
DEVI u32x2 ldstr(LAS unsigned char* L, int off) {
  return __builtin_bit_cast(u32x2, __builtin_amdgcn_ds_read_tr16_b64_v4i16((LAS s16x4*)(L + off)));
}
DEVI float ror8(float v) { return asf(__builtin_amdgcn_mov_dpp((int)asu(v), 0x128, 0xf, 0xf, true)); }
constexpr int SC1 = 16;
constexpr u32 OOB = 0x7ffffff0u;
DEVI void st128(u32x4 v, __amdgpu_buffer_rsrc_t rs, u32 off) { __builtin_amdgcn_raw_buffer_store_b128(v, rs, off, 0, SC1); }
DEVI void st_flag(u32x2 tag, __amdgpu_buffer_rsrc_t rs, u32 off, int lane) { __builtin_amdgcn_raw_buffer_store_b64(tag, rs, lane == 0 ? off : OOB, 0, SC1); }
#define MK_RWS() __builtin_amdgcn_make_buffer_rsrc((void*)ws, 0, (int)WS_END, 0x00020000)
template <int V> struct IC { static constexpr int value = V; };
template <int I, int N, class F> DEVI void sfor(F&& f) { if constexpr (I < N) { f(IC<I>{}); sfor<I + 1, N>(f); } }
#define AINL __attribute__((always_inline))
template <class T> DEVI void pin(T& v) { asm volatile("" : "+v"(v)); }
DEVI float sfma(float a, float b, float c) { float r = __builtin_fmaf(a, b, c); asm("" : "+v"(r)); return r; }
DEVI float smul(float a, float b) { float r = a * b; asm("" : "+v"(r)); return r; }
DEVI float sadd(float a, float b) { float r = a + b; asm("" : "+v"(r)); return r; }
#define SGB(mask, n) __builtin_amdgcn_sched_group_barrier(mask, n, 0)
DEVI u32 umax(u32 a, u32 b) { return a > b ? a : b; }
DEVI u32 max4(u32x4 v) { return umax(umax(v[0], v[1]), umax(v[2], v[3])); }
DEVI bool any_sent(u32 m) { return __builtin_amdgcn_ballot_w64(m == SENT) != 0ull; }

#define STAMP(n)
#define STAMP_DECL
#define STAMP_OUT()

#define STAMPX(n) STAMP(n)
DEVI void prog_set(LAS unsigned char* L, int w, int lane, u32 v) {
  FENCE(); *(volatile LAS u32*)(L + LDS_PROG + w * 256 + lane * 4) = v; FENCE();
}
DEVI int pga_of(int first, int cnt, int lane) { int a = LDS_PROG + (first + (lane & (cnt - 1))) * 260; pin(a); return a; }
DEVI u32 prog_get(LAS unsigned char* L, int pga) { return *(volatile LAS u32*)(L + pga); }
DEVI bool prog_bad(u32 v, u32 target) { return __builtin_expect(__builtin_amdgcn_ballot_w64(v < target) != 0ull, 0); }
template <int SL = 0> DEVI void prog_spin(LAS unsigned char* L, int pga, u32 target) {
  NOUNROLL
  for (int s = 0; s < SPIN_CAP * 8; ++s) { if (SL) __builtin_amdgcn_s_sleep(SL); if (__builtin_amdgcn_ballot_w64(prog_get(L, pga) < target) == 0ull) break; }
  FENCE();
}
template <int SL = 0> DEVI void prog_wait(LAS unsigned char* L, int pga, u32 target) {
  if (prog_bad(prog_get(L, pga), target)) prog_spin<SL>(L, pga, target);
  FENCE();
}
template <int NL, int SL> DEVI void flag_wait(__amdgpu_buffer_rsrc_t rs, u32 off, int lane, int nlanes, u32x2 tag) {
  bool miss[NL];
#pragma unroll
  for (int n = 0; n < NL; ++n) miss[n] = lane < nlanes;
  NOUNROLL
  for (int s = 0; s < SPIN_CAP; ++s) {
    FENCE();
    u32x2 f[NL];
#pragma unroll
    for (int n = 0; n < NL; ++n) f[n] = __builtin_amdgcn_raw_buffer_load_b64(rs, miss[n] ? off + (n * 64 + lane) * 128 : 0x7ffffff0u, 0, 16);
    bool any = false;
#pragma unroll
    for (int n = 0; n < NL; ++n) { miss[n] = miss[n] && !(f[n][0] == tag[0] && f[n][1] == tag[1]); any |= miss[n]; }
    if (__builtin_amdgcn_ballot_w64(any) == 0ull) break;
    __builtin_amdgcn_s_sleep(SL);
  }
  FENCE();
}

DEVI void gen32(float a0, float step, float K, u32x4* o) {
  f32x2 p = {cosrev(a0), cosrev(a0 + step)}, q = {cosrev(a0 + 2.f * step), cosrev(a0 + 3.f * step)};
  o[0][0] = pk_bf16v(p); o[0][1] = pk_bf16v(q);
#pragma unroll
  for (int m = 2; m < 16; ++m) { const f32x2 r = K * q - p; p = q; q = r; o[m >> 2][m & 3] = pk_bf16v(r); }
}

#define TFSTEP 5
#define CODE_LINES 352
#define TPOLL 9
#define W2STOP 3
#define WLOADAT 3
#define J3SH 0
#define TSLEEP 8

#define BID __builtin_amdgcn_workgroup_id_x()
#define TID __builtin_amdgcn_workitem_id_x()
struct Args { const u16* x; const u16* wr; const u16* wi; const u16* w1; const u16* w2; unsigned char* ws; u16* out; };
#define CAS __attribute__((address_space(4)))
__global__ __launch_bounds__(512, 2) void k_all(Args args_unused) {
  extern __shared__ __attribute__((aligned(16))) unsigned char smem[];
  LAS unsigned char* L = (LAS unsigned char*)smem;
  const int tid = TID, lane = tid & 63, w = __builtin_amdgcn_readfirstlane(tid >> 6), g = lane >> 4, c = lane & 15;
  u64 pc0; asm volatile("s_getpc_b64 %0" : "=s"(pc0));
  STAMP_DECL STAMP(0);
  *(volatile LAS u32*)(L + LDS_PROG + tid * 4) = 0u;
  BAR0();
  const CAS Args* ap = (const CAS Args*)__builtin_amdgcn_kernarg_segment_ptr();
  asm volatile("" : "+s"(ap));
#define x (ap->x)
#define wr (ap->wr)
#define wi (ap->wi)
#define w1 (ap->w1)
#define w2 (ap->w2)
#define ws (ap->ws)
#define out (ap->out)
  const int xc = BID & 7, idx = BID >> 3;
  const int i = 8 * (xc >> 1) + (idx >> 2), j = 4 * (xc & 1) + (idx & 3);
  const int i3 = 4 * xc + (idx >> 3), j3 = (idx + J3SH) & 7;
  const int rot2 = (5 * (idx & 7) + 11 * (idx >> 3)) % 43, rotA = 2 * (idx & 7);
  const int rotx = idx & 3, rot1 = 2 * (idx >> 2);
  const int u0 = 11 * j - (j == 7), nt = 11 - (j >= 6);

  if (__builtin_expect(w >= 4 && w < 6, 1)) {
    __builtin_amdgcn_s_setprio(3);
    const int l = w - 4;
    const u32 lbase = (u32)(size_t)L;
    const u32x4 sx = mk_srd(x, S_ * D_ * 2), sw1 = mk_srd((const char*)w1 - 4096, D_ * F_ * 2 + 4096), sw2 = mk_srd(w2, D_ * F_ * 2);
#define X_ISSUE(t) dma_x8(sx, lane * 16, (32 * ((16 * (t) + 8 * l + 16 * rotx) & 63) + i) * 1024, lbase + LDS_X + (16 * (t) + 8 * l) * XROW)
    X_ISSUE(0); X_ISSUE(1); X_ISSUE(2);
    WAITV(16); prog_set(L, w, lane, 1);
    X_ISSUE(3);
    WAITV(16); prog_set(L, w, lane, 2);
    SCHED();
    u32 w1off[6];
#pragma unroll
    for (int n = 0; n < 6; ++n) {
      const int e = n * 64 + lane, rr = (e * 745) >> 14; int ch = e - rr * 22;
      if (ch >= 2 * nt) ch -= 2;
      w1off[n] = (rr & 3) * (F_ * 2) + ((rr >> 2) & 3) * (128 * F_ * 2) + ch * 16 + (4 * l * (F_ * 2) + u0 * 32 + 4096 - (n & 3) * 1024);
      pin(w1off[n]);
    }
    SCHED();
    WAITV(8); prog_set(L, w, lane, 3);
    STAMP(1);
    const int gC = pga_of(0, 4, lane);
    {
      u32 kk = rot1, pv = 0;
      const u32 d0 = lbase + LDS_RING + l * 5632;
      u32 w2off[4];
      const __amdgpu_buffer_rsrc_t rw2 = __builtin_amdgcn_make_buffer_rsrc((void*)w2, 0, D_ * F_ * 2, 0x00020000);
      u32x4 stg[52];
      int mdone = 0;
      u32 so2 = (rot2 + l + 2 * U2N) * (32 * D_ * 2); if (so2 >= 43 * 32 * D_ * 2) so2 -= 43 * 32 * D_ * 2;
#define W2STG(m) do { _Pragma("unroll") for (int q = 0; q < 4; ++q) stg[(m) * 4 + q] = __builtin_amdgcn_raw_buffer_load_b128(rw2, w2off[q] + q * 1024, so2, 0); \
        so2 += 2 * 32 * D_ * 2; if (so2 >= 43 * 32 * D_ * 2) so2 -= 43 * 32 * D_ * 2; } while (0)
#pragma unroll
      for (int n = 0; n < 16; ++n) {
        if (n >= NSLOT) { if (prog_bad(pv, PC_W1 + (n - NSLOT))) prog_spin<1>(L, gC, PC_W1 + (n - NSLOT)); }
        const u32 so = kk * (8 * F_ * 2);
        dma_4k(sw1, w1off[0], w1off[1], w1off[2], w1off[3], so, d0 + ((n + 5) % NSLOT) * W1CH);
        kk = (kk + 1) & 15;
        dma_w1tail(sw1, w1off[4], w1off[5], so);
        if (n >= NSLOT && n + 1 < 16) pv = prog_get(L, gC);
        if (n == 0) { WAITV(6); prog_set(L, w, lane, 4); STAMP(7); }
        else if (n == 6) { WAITV(12); prog_set(L, w, lane, 3 + n); STAMP(8); }
        else if (n >= 2 && n < NSLOT) { WAITV(12); prog_set(L, w, lane, 3 + n); }
        else if (n >= NSLOT + 3) { WAITV(18); prog_set(L, w, lane, 2 + n); }
        if (n == NSLOT - 1) {
#pragma unroll
          for (int q = 0; q < 4; ++q) {
            const int row = 8 * q + (lane >> 3), pos = lane & 7, swz = ((row >> 1) & 1) | (((row >> 3) & 1) << 1);
            w2off[q] = row * (D_ * 2) + 128 * j3 + ((((pos >> 1) ^ swz) << 1) | (pos & 1)) * 16 - q * 1024;
            pin(w2off[q]);
          }
          WAITV(6); prog_set(L, w, lane, 4 + n);
#pragma unroll
          for (int m = 0; m < 13; ++m) {
            if (mdone == m) {
              pv = prog_get(L, gC);
              W2STG(m);
              if (m == 0) { WAITV(4); prog_set(L, w, lane, 5 + n); } else { WAITV(W2WIN); }
              if (__builtin_amdgcn_ballot_w64(pv < W2STOP) != 0ull) mdone = m + 1; else mdone = m + 101;
            }
          }
          if (mdone > 100) mdone -= 100;
          STAMP(6);
          pv = prog_get(L, gC);
        }
      }
      WAITV(12); prog_set(L, w, lane, PL_W1 + 13);
      WAITV(6); prog_set(L, w, lane, PL_W1 + 14);
      STAMP(2);
      WAITV(0); prog_set(L, w, lane, PL_W1 + 15);
#pragma unroll
      for (int m = 1; m < 13; ++m) if (m >= mdone) { W2STG(m); WAITV(W2WIN); }
      u32 so = (rot2 + l) * (32 * D_ * 2); if (so >= 43 * 32 * D_ * 2) so -= 43 * 32 * D_ * 2;
      const u32 d2 = lbase + l * 4096;
#pragma unroll
      for (int n = 0; n < U2N; ++n) {
        prog_wait<1>(L, gC, PC_W1 + 9 + (n * 8192 + 8191) / W1CH);
        dma_4k(sw2, w2off[0], w2off[1], w2off[2], w2off[3], so, d2 + u2slot(n));
        WAITV(W2WIN);
        so += 2 * 32 * D_ * 2; if (so >= 43 * 32 * D_ * 2) so -= 43 * 32 * D_ * 2;
      }
      STAMP(4);
      WAITV(0); prog_set(L, w, lane, PL_W2 + U2N - 1);
      STAMP(5);
      int wa0 = LDS_RING + l * 4096 + lane * 16, wa1 = wa0 + 4 * 8192; pin(wa0); pin(wa1);
#pragma unroll
      for (int n = U2N; n < 22; ++n) {
        prog_wait<1>(L, gC, pcw2(n - U2N));
#pragma unroll
        for (int q = 0; q < 4; ++q) *(LAS u32x4*)(L + ((n % U2N) < 4 ? wa0 + (n % U2N) * 8192 : wa1 + ((n % U2N) - 4) * 8192) + q * 1024) = stg[(n - U2N) * 4 + q];
        prog_set(L, w, lane, PL_W2 + n);
      }
    }
    STAMP(3);
    STAMP_OUT();
    return;
  }

  if (w >= 6) {
    const int q = w - 6;
    const __amdgpu_buffer_rsrc_t rws = MK_RWS();
    const int p = (i & ~1) + q, hq = i & 1;
    const int gC = pga_of(0, 4, lane), gP = pga_of(6, 2, lane), gP0 = pga_of(6, 1, lane);
    u32x2 ep = {0u, 0u};
    if (q == 0) ep = __builtin_amdgcn_raw_buffer_load_b64(rws, WS_EPOCH, 0, SC1);
    if (q == 1) {
      const int ln = idx * 16 + c;
      if (lane < 16 && ln < CODE_LINES) { const u32 v = *(const volatile GAS u32*)(pc0 + (u64)ln * 128); asm volatile("" :: "v"(v)); }
    }
    u32x2 tag;
    {
      SCHED();
      prog_wait<2>(L, pga_of(4, 2, lane), WLOADAT);
      const bool spec = j == 0 && hq == 0;
      u32 raw[8][4], nyr = 0, nyi = 0;
      {
        const __amdgpu_buffer_rsrc_t rwr = __builtin_amdgcn_make_buffer_rsrc((void*)wr, 0, S_ * DF * 2, 0x00020000);
        const __amdgpu_buffer_rsrc_t rwi = __builtin_amdgcn_make_buffer_rsrc((void*)wi, 0, S_ * DF * 2, 0x00020000);
        const int colb = (32 * j + 16 * hq + c) * 2;
        const int vp = 128 * g * (DF * 2) + colb, vm = (384 - 128 * g) * (DF * 2) + colb;
        const int vm0 = ((p == 0 && g == 0) ? 1024 : 2048 - p - 128 * g) * (DF * 2) + colb;
#define W_LOADS(dst, dc) _Pragma("unroll") for (int e = 0; e < 8; ++e) { \
          const int ku = p + 512 * (e >> 1) + 64 * (e & 1) - ((e >= 4 && p == 0) ? 992 : 0); \
          dst[e][0] = __builtin_amdgcn_raw_buffer_load_b16(rwr, vp + (dc), ku * (DF * 2), 0); \
          dst[e][1] = __builtin_amdgcn_raw_buffer_load_b16(rwi, vp + (dc), ku * (DF * 2), 0); \
          dst[e][2] = __builtin_amdgcn_raw_buffer_load_b16(rwr, (e == 0 ? vm0 : vm) + (dc), e == 0 ? 0 : (1664 - ku) * (DF * 2), 0); \
          dst[e][3] = __builtin_amdgcn_raw_buffer_load_b16(rwi, (e == 0 ? vm0 : vm) + (dc), e == 0 ? 0 : (1664 - ku) * (DF * 2), 0); }
        W_LOADS(raw, 0)
        if (spec) {
          const int e = c >> 1, kp = p + 512 * (e >> 1) + 64 * (e & 1) + 128 * g - ((e >= 4 && p == 0) ? 992 : 0);
          const int kr = (c & 1) ? (kp == 0 ? 1024 : 2048 - kp) : kp;
          nyr = __builtin_amdgcn_raw_buffer_load_b16(rwr, kr * (DF * 2) + 512, 0, 0);
          nyi = __builtin_amdgcn_raw_buffer_load_b16(rwi, kr * (DF * 2) + 512, 0, 0);
        }
      }
      SCHED();
      u32x4 mf[4][2], mi[4][2];
      const int pq = c & 1;
#pragma unroll
      for (int t = 0; t < 4; ++t) {
        const int phi = 8 * t + (c >> 1);
        int k = p + 64 * phi; float ph0 = -0.25f * (float)pq, ph1 = ph0 + 0.25f, m0 = 1.f, m1 = 1.f;
        if (p == 0) {
          ph1 = ph0;
          if (t < 2) m1 = 0.f; else { m0 = 0.f; k -= 992; }
          if (t == 0 && c == 1) { k = 1024; ph0 = 0.f; }
        }
        const float kf = (float)k * (1.f / 2048.f), a = (float)g * kf, b = a + 4.f * kf, K = 2.f * cosrev(4.f * kf);
        float u0 = smul(cosrev(sadd(a, ph0)), m0), v0 = smul(cosrev(sadd(a, ph1)), m1), u1 = smul(cosrev(sadd(b, ph0)), m0), v1 = smul(cosrev(sadd(b, ph1)), m1);
        mf[t][0][0] = pk_f16(u0, v0); mf[t][0][1] = pk_f16(u1, v1);
#pragma unroll
        for (int n = 2; n < 8; ++n) {
          const float u2 = sfma(K, u1, -u0), v2 = sfma(K, v1, -v0); u0 = u1; u1 = u2; v0 = v1; v1 = v2;
          mf[t][n >> 2][n & 3] = pk_f16(u2, v2);
        }
      }
#pragma unroll
      for (int t = 0; t < 4; ++t) { pin(mf[t][0]); pin(mf[t][1]); }
      SCHED();
      if (q == 0) {
        const u64 t = (((u64)ep[1] << 32) | ep[0]) + 1;
        if (i == 0) {
          const u64 b0 = t << 8;
          __builtin_amdgcn_raw_buffer_store_b64(u32x2{(u32)b0, (u32)(b0 >> 32)}, rws, lane < 2 ? WS_CV + (j * 2 + lane) * 256 : OOB, 0, SC1);
          WAITV(0);
        }
        *(LAS u32x2*)(L + LDS_TAG + lane * 8) = u32x2{(u32)t, (u32)(t >> 32)};
        prog_set(L, w, lane, 1);
      }
      SCHED();
      {
        const int which = c & 1, kq = (c >> 1) & 3;
        const u32 msk0 = (p == 0 && which) ? 0u : ~0u, msk1 = (p == 0 && !which) ? 0u : ~0u;
        const u32 sgn = (p != 0 && (c & 2)) ? 0x80008000u : 0u;
        const u32 rsh = (kq & 1) * 16, rsg = kq == 0 ? 0u : kq == 1 ? 0x00008000u : kq == 2 ? 0x80008000u : 0x80000000u;
#pragma unroll
        for (int nu = 0; nu < 4; ++nu) {
          const float s2 = (float)(8 * nu + (c >> 1));
          const float beta = p != 0 ? sfma(s2, (float)p * (1.f / 2048.f), 0.25f * (float)which) : (which ? s2 * (1.f / 64.f) : 0.f);
#pragma unroll
          for (int r = 0; r < 2; ++r) {
            const float a = sfma(s2, (float)(2 * g + r) * (1.f / 32.f), beta);
            u32 v = pk_bf16(smul(cosrev(a), 0x1p-14f), smul(cosrev(sadd(a, -0.25f)), 0x1p-14f));
            const u32 v2 = __builtin_amdgcn_alignbit(v, v, rsh) ^ rsg;
            if (r == 0 && p == 0 && g == 0 && !which) v = (v & 0xffffu) | ((c & 2) ? 0xb8800000u : 0x38800000u);
            mi[nu][0][r] = v & msk0; mi[nu][1][r] = (v ^ sgn) & msk1;
            mi[nu][0][r + 2] = v2 & msk0; mi[nu][1][r + 2] = (v2 ^ sgn) & msk1;
          }
        }
      }
#pragma unroll
      for (int t = 0; t < 4; ++t) { pin(mi[t][0]); pin(mi[t][1]); }
      SCHED();
      STAMP(1);
      float ca[8], cb[8], cc[8], cd[8], ca2[8], cc2[8], aO, bO, aO2;
#pragma unroll
      for (int e = 0; e < 8; ++e) {
        const float rp = bf2f(raw[e][0]), ip = bf2f(raw[e][1]), rm = bf2f(raw[e][2]), im = bf2f(raw[e][3]);
        ca[e] = sadd(rp, rm); cb[e] = sadd(ip, im); cc[e] = sadd(im, -ip); cd[e] = sadd(rp, -rm);
        if (e == 0) {
          aO = ca[0]; bO = cb[0];
          if (p == 0 && g == 0) { ca[0] = rp; cb[0] = ip; cc[0] = 0.f; cd[0] = 0.f; aO = rm; bO = im; }
          aO2 = aO;
        }
        ca2[e] = ca[e]; cc2[e] = cc[e];
      }
      if (spec) {
        const bool sl = c == 0;
        const float hf = sl ? 0.5f : 1.f, zf = sl ? 0.f : 1.f;
        const float r0 = smul(bf2f(nyr), 0.5f), i0 = smul(bf2f(nyi), 0.5f);
        const float r1 = asf(__builtin_amdgcn_mov_dpp((int)asu(r0), 0xb1, 0xf, 0xf, true)), i1 = asf(__builtin_amdgcn_mov_dpp((int)asu(i0), 0xb1, 0xf, 0xf, true));
        float na = sadd(r0, r1), nc = sadd(i1, -i0), no = na;
        if (p == 0 && g == 0 && sl) { na = r0; nc = 0.f; no = r1; }
        sfor<0, 8>([&](auto E_) AINL {
          constexpr int e = decltype(E_)::value;
          const float va = e == 0 ? na : asf(__builtin_amdgcn_mov_dpp((int)asu(na), 0x100 + 2 * e, 0xf, 0xf, true));
          const float vc = e == 0 ? nc : asf(__builtin_amdgcn_mov_dpp((int)asu(nc), 0x100 + 2 * e, 0xf, 0xf, true));
          ca2[e] = sl ? va : ca2[e]; cc2[e] = sl ? vc : cc2[e];
          ca[e] = smul(ca[e], hf); cc[e] = smul(cc[e], hf); cb[e] = smul(cb[e], zf); cd[e] = smul(cd[e], zf);
        });
        aO2 = sl ? no : aO2; aO = smul(aO, hf); bO = smul(bO, zf);
      }
#pragma unroll
      for (int e = 0; e < 8; ++e) { pin(ca[e]); pin(cb[e]); pin(cc[e]); pin(cd[e]); pin(ca2[e]); pin(cc2[e]); }
      pin(aO); pin(bO); pin(aO2);
      const int aoff = WS_A + (((16 * q + (lane >> 4)) * 4 + (p >> 3)) * 16 + ((lane >> 3) & 1) * 8 + j) * 1024 +
                       ((2 * hq + ((lane >> 2) & 1)) * 16 + 2 * (p & 6) + (lane & 3)) * 16;
      prog_wait<1>(L, gP0, 1);
      tag = *(LAS u32x2*)(L + LDS_TAG + lane * 8);
      STAMP(2);
      __builtin_amdgcn_s_setprio(2);
      if (q == 0) {
        prog_wait<2>(L, gC, PC_W1 + TPOLL);
        STAMP(3);
        flag_wait<1, 4>(rws, WS_FT + (j * 4 + 2 * hq) * 32 * 128, lane, 64, tag);
        prog_set(L, w, lane, 2);
      } else prog_wait<1>(L, gP0, 2);
      STAMP(5);
      u32 tb[2][8];
#pragma unroll
      for (int n = 0; n < 8; ++n)
#pragma unroll
        for (int tl = 0; tl < 2; ++tl)
          tb[tl][n] = __builtin_amdgcn_raw_buffer_load_b32(rws, WS_T + ((j * 32 + p) * 32 + g) * 256 + hq * 128 + tl * 64 + c * 4, n * 1024, SC1);
      SCHED();
      f32x4 pqv[2][4];
#pragma unroll
      for (int t = 0; t < 4; ++t)
#pragma unroll
        for (int tl = 0; tl < 2; ++tl) {
          const u32x4 b0 = {tb[tl][0], tb[tl][1], tb[tl][2], tb[tl][3]}, b1 = {tb[tl][4], tb[tl][5], tb[tl][6], tb[tl][7]};
          pqv[tl][t] = mfma_h(mf[t][0], b0, f32x4{0.f, 0.f, 0.f, 0.f}); pqv[tl][t] = mfma_h(mf[t][1], b1, pqv[tl][t]);
        }
      STAMP(6);
      u32x4 eo[2][2];
#pragma unroll
      for (int e = 0; e < 8; ++e) {
        const float Pr = pqv[0][e >> 1][2 * (e & 1)], Qr = pqv[0][e >> 1][2 * (e & 1) + 1], Pi = pqv[1][e >> 1][2 * (e & 1)], Qi = pqv[1][e >> 1][2 * (e & 1) + 1];
        const float a_o = e == 0 ? aO : ca[e], b_o = e == 0 ? bO : cb[e], a_o2 = e == 0 ? aO2 : ca2[e];
        float Er = smul(Pr, ca[e]), Ei = smul(Pi, ca2[e]), Or = smul(Pr, cc[e]), Oi = smul(Pi, cc2[e]);
        Er = sfma(Pi, -cb[e], Er); Ei = sfma(Pr, cb[e], Ei); Or = sfma(Pi, -cd[e], Or); Oi = sfma(Pr, cd[e], Oi);
        Er = sfma(Qr, -cc[e], Er); Ei = sfma(Qi, -cc2[e], Ei); Or = sfma(Qr, a_o, Or); Oi = sfma(Qi, a_o2, Oi);
        Er = sfma(Qi, cd[e], Er); Ei = sfma(Qr, -cd[e], Ei); Or = sfma(Qi, -b_o, Or); Oi = sfma(Qr, b_o, Oi);
        eo[0][e >> 2][e & 3] = pk_bf16(Er, Or); eo[1][e >> 2][e & 3] = pk_bf16(Ei, Oi);
        SCHED();
        if (e == 3) {
#pragma unroll
          for (int tl = 0; tl < 2; ++tl)
#pragma unroll
            for (int nu = 0; nu < 2; ++nu) pqv[tl][nu] = mfma_bf(eo[tl][0], mi[nu][0], f32x4{0.f, 0.f, 0.f, 0.f});
          SCHED();
        }
      }
#pragma unroll
      for (int tl = 0; tl < 2; ++tl)
#pragma unroll
        for (int nu = 0; nu < 2; ++nu) pqv[tl][nu] = mfma_bf(eo[tl][1], mi[nu][1], pqv[tl][nu]);
#pragma unroll
      for (int tl = 0; tl < 2; ++tl)
#pragma unroll
        for (int nu = 2; nu < 4; ++nu) { pqv[tl][nu] = mfma_bf(eo[tl][0], mi[nu][0], f32x4{0.f, 0.f, 0.f, 0.f}); pqv[tl][nu] = mfma_bf(eo[tl][1], mi[nu][1], pqv[tl][nu]); }
      {
        const int sa = LDS_TT + (((c >> 1) * 4 + (g >> 1)) * 4 + 2 * q + (c & 1)) * 16 + (g & 1) * 8;
#pragma unroll
        for (int nu = 0; nu < 4; ++nu)
#pragma unroll
          for (int tl = 0; tl < 2; ++tl)
            *(LAS u32x2*)(L + sa + nu * 2048 + tl * 128) = u32x2{pk_bf16(pqv[tl][nu][0], pqv[tl][nu][1]), pk_bf16(pqv[tl][nu][2], pqv[tl][nu][3])};
      }
      prog_set(L, w, lane, 3);
      prog_wait(L, gP, 3);
#pragma unroll
      for (int n = 0; n < 4; ++n) {
        const u32x4 v = lds128(L, LDS_TT + ((q * 4 + n) * 64 + lane) * 16);
        st128(v, rws, aoff + n * 262144);
      }
      STAMP(7);
      WAITV(0);
      if (lane == 0) {
        const u64 old = __hip_atomic_fetch_add((GAS u64*)(ws + WS_CV + (j * 2 + q) * 256), 1ull, __ATOMIC_RELAXED, __HIP_MEMORY_SCOPE_AGENT);
        if ((old & 0xffull) == 31ull) __builtin_amdgcn_raw_buffer_store_b64(tag, rws, WS_DV + (q * 8 + j) * 128, 0, SC1);
      }
    }
    STAMP(8);
    prog_set(L, w, lane, 4);
    STAMP(9);
    if (q == 0) {
      flag_wait<1, 4>(rws, WS_DV + (i3 >> 4) * 8 * 128, lane, 8, tag);
      prog_set(L, w, lane, 5);
      if (BID == 0 && lane == 0) __builtin_amdgcn_raw_buffer_store_b64(tag, rws, WS_EPOCH, 0, SC1);
    }
    STAMP(11);
    {
      const u32 aoff = WS_A + (i3 * 4 + 2 * q) * 16384 + lane * 16;
      if (q == 1) prog_wait<1>(L, gP0, 5);
      constexpr int AW = 6;
      u32x4 a0[AW], a1[AW], fi[8];
      f32x4 pm[2][4];
#define A_LOAD(ks) do { a0[(ks) % AW] = __builtin_amdgcn_raw_buffer_load_b128(rws, aoff, (((ks) + rotA) & 15) * 1024, SC1); \
                        a1[(ks) % AW] = __builtin_amdgcn_raw_buffer_load_b128(rws, aoff + 16384, (((ks) + rotA) & 15) * 1024, SC1); } while (0)
#pragma unroll
      for (int ks = 0; ks < AW; ++ks) { A_LOAD(ks); SCHED(); }
      prog_wait<1>(L, gC, PC_FI); prog_wait<1>(L, gP, 4);
#pragma unroll
      for (int n = 0; n < 8; ++n) fi[n] = lds128(L, LDS_FI + n * 1024 + lane * 16);
      SCHED();
#pragma unroll
      for (int n = 0; n < 64; ++n) {
        const int ks = n >> 2;
        MFA(n < 4, pm[0][n & 3], a0[ks % AW], fi[n & 7]);
        SCHED();
        MFA(n < 4, pm[1][n & 3], a1[ks % AW], fi[n & 7]);
        if (n + 8 < 64) fi[n & 7] = lds128(L, LDS_FI + (n + 8) * 1024 + lane * 16);
        if ((n & 3) == 3 && ks + AW < 16) A_LOAD(ks + AW);
        SCHED();
      }
      MFMA_SETTLE();
#pragma unroll
      for (int tt = 0; tt < 2; ++tt)
#pragma unroll
        for (int nu = 0; nu < 4; ++nu)
          *(LAS u32x2*)(L + LDS_PM + (16 * nu + c) * PMROW + (16 * (2 * q + tt) + 4 * g) * 2) =
              u32x2{pk_bf16(pm[tt][nu][0], pm[tt][nu][1]), pk_bf16(pm[tt][nu][2], pm[tt][nu][3])};
      prog_set(L, w, lane, 6);
    }
    STAMP(12);
    STAMP_OUT();
    return;
  }

  u32x4 Ff[16];
  {
    int kd = 32 * j + 16 * (w >> 1) + c; float ph = (w & 1) ? 0.25f : 0.f;
    if (kd == 0 && (w & 1)) { kd = 256; ph = -0.25f; }
    const float step = (float)kd * (1.f / 512.f), K = 2.f * cosrev(2.f * step);
#pragma unroll
    for (int a = 0; a < 4; ++a) {
      const int k0 = 128 * g + 32 * ((a + w) & 3);
      gen32((float)(((2 * k0 + 1) * kd) & 1023) * (1.f / 1024.f) + ph, step, K, &Ff[4 * a]);
    }
  }
#pragma unroll
  for (int n = 0; n < 16; ++n) pin(Ff[n]);
  STAMP(1);
  const f32x4 z4 = {0.f, 0.f, 0.f, 0.f};
  const int gC = pga_of(0, 4, lane), gL = pga_of(4, 2, lane);
#define gP pga_of(6, 2, lane)
#define gP0 pga_of(6, 1, lane)
  f32x4 xa[4];
  u32x4 Gf[4][2];
  {
    f32x4 sacc, qacc[4];
    int abase[4];
#pragma unroll
    for (int a = 0; a < 4; ++a) { abase[a] = LDS_X + c * XROW + (16 * g + 4 * ((a + w) & 3)) * 16; pin(abase[a]); }
    const float gph = (c & 1) ? -0.25f : 0.f, gk0 = (float)(c >> 1) * (1.f / 64.f), gs0 = (float)(4 * g + 16 * rotx);
    const u32 gsg = (c & 2) ? 0x80008000u : 0u;
    int so_ = c < 4 ? LDS_ST + w * 256 + (16 * c + 4 * g) * 4 : LDS_DUMMY + lane * 16;
    int qo_ = (c >> 2) == g ? LDS_ST + 1024 + w * 256 + c * 4 : LDS_DUMMY + lane * 4;
    pin(so_); pin(qo_);
    u32x4 sel[4];
#pragma unroll
    for (int t = 0; t < 4; ++t) { const u32 sv = c == t ? 0x3f803f80u : 0u; sel[t] = u32x4{sv, sv, sv, sv}; pin(sel[t]); }
#define GGEN(id) do { const int gt = (id) >> 2, gr = (id) & 3; const float kf = gk0 + (float)gt * 0.125f; \
      const float ga = sfma(gs0 + (float)(16 * (gr >> 1) + 2 * (gr & 1)), kf, gph); \
      u32 v = pk_f16(cosrev(ga), cosrev(sadd(ga, kf))); if (gt == 0 && c == 1) v = 0xbc003c00u; \
      u32 v2 = v ^ gsg; pin(v); pin(v2); Gf[gt][0][gr] = v; Gf[gt][1][gr] = v2; } while (0)
    GGEN(0); GGEN(1); GGEN(2); GGEN(3); GGEN(4); GGEN(5);
    u32x4 af[16];
    prog_wait(L, gL, 1);
    STAMP(2);
    u32 pv = prog_get(L, gL);
#pragma unroll
    for (int n = 0; n < 16; ++n) af[n] = lds128(L, abase[n >> 2] + (n & 3) * 16);
    sfor<0, 4>([&](auto T_) AINL {
      constexpr int t = decltype(T_)::value;
      if (t == 0) { SCHED(); GGEN(6); GGEN(7); GGEN(8); GGEN(9); GGEN(10); }
      if (t < 3) { if (prog_bad(pv, t + 2)) prog_spin(L, gL, t + 2); }
      SCHED();
#pragma unroll
      for (int n = 0; n < 16; ++n) {
        if (n < 4) { MFA(t == 0 && n == 0, sacc, af[n], sel[t]); MFA(n == 0, qacc[t], af[n], af[n]); }
        MFA(n == 0, xa[t], af[n], Ff[n]);
        if (t < 3) af[n] = lds128(L, abase[n >> 2] + (n & 3) * 16 + (t + 1) * 16 * XROW);
        if (t < 2 && n == 3) pv = prog_get(L, gL);
        if (n == 2 && t > 0) {
          const float lo = (c & 1) ? qacc[(t + 3) & 3][1] : qacc[(t + 3) & 3][0], hi = (c & 1) ? qacc[(t + 3) & 3][3] : qacc[(t + 3) & 3][2];
          *(LAS float*)(L + qo_ + (t - 1) * 64) = (c & 2) ? hi : lo;
        }
        if (t == 3 && n == 7) {
          *(LAS f32x4*)(L + so_) = sacc;
          const float lo = (c & 1) ? qacc[3][1] : qacc[3][0], hi = (c & 1) ? qacc[3][3] : qacc[3][2];
          *(LAS float*)(L + qo_ + 3 * 64) = (c & 2) ? hi : lo;
          prog_set(L, w, lane, PC_STAT);
        }
        if (t == 3 && n >= 8 && n < 13) GGEN(n + 3);
        SCHED();
      }
    });
  }
  STAMP(3);
  prog_wait(L, gC, PC_STAT);
  {
    float sm = 0.f, sq = 0.f;
#pragma unroll
    for (int a = 0; a < 4; ++a) { sm += *(LAS float*)(L + LDS_ST + a * 256 + lane * 4); sq += *(LAS float*)(L + LDS_ST + 1024 + a * 256 + lane * 4); }
    const float mean = sm * (1.f / 512.f);
    const float var = fmaxf(sq * (1.f / 512.f) - mean * mean, 0.f);
    const float sd = rbf(__builtin_amdgcn_sqrtf(rbf(rbf(var) + 1e-5f)));
    *(LAS float*)(L + LDS_ST2 + w * 512 + lane * 4) = rbf(mean);
    *(LAS float*)(L + LDS_ST2 + w * 512 + 256 + lane * 4) = __builtin_amdgcn_rcpf(sd);
    FENCE();
  }
  {
    u32x4 xb[2];
    f32x4 rv[4];
#pragma unroll
    for (int t = 0; t < 4; ++t) rv[t] = *(LAS f32x4*)(L + LDS_ST2 + w * 512 + 256 + (16 * t + 4 * g) * 4);
    if (j == 0 && w == 0) {
      const float mco = c == 0 ? 512.f : 0.f;
#pragma unroll
      for (int t = 0; t < 4; ++t) xa[t] -= mco * *(LAS f32x4*)(L + LDS_ST2 + w * 512 + (16 * t + 4 * g) * 4);
    }
#pragma unroll
    for (int t = 0; t < 4; ++t) {
      const f32x4 v = xa[t] * (rv[t] * 0.03125f);
      xb[t >> 1][2 * (t & 1)] = pk_f16(v[0], v[1]);
      xb[t >> 1][2 * (t & 1) + 1] = pk_f16(v[2], v[3]);
    }
    f32x4 tt[4];
#pragma unroll
    for (int t = 0; t < 4; ++t) tt[t] = mfma_h(Gf[t][0], xb[0], z4);
#pragma unroll
    for (int t = 0; t < 4; ++t) tt[t] = mfma_h(Gf[t][1], xb[1], tt[t]);
#pragma unroll
    for (int t = 0; t < 4; ++t)
#pragma unroll
      for (int sub = 0; sub < 2; ++sub)
        *(LAS u32*)(L + LDS_TT + w * 2048 + (8 * t + 2 * g + sub) * 64 + c * 4) = pk_f16(tt[t][2 * sub], tt[t][2 * sub + 1]);
  }
  FENCE();
  const u32x4 tv0 = lds128(L, LDS_TT + w * 2048 + lane * 16), tv1 = lds128(L, LDS_TT + w * 2048 + 1024 + lane * 16);
  const __amdgpu_buffer_rsrc_t rws = MK_RWS();
  const u32 toff = WS_T + ((j * 32 + (lane >> 2)) * 32 + i) * 256 + w * 64 + (lane & 3) * 16;
  u32x2 tag;
  STAMP(4);
  f32x4 acc[3][5];
  {
    const int xbase = LDS_X + c * XROW + 16 * g * 16;
    int wbA[2], wbB[2];
    wbA[0] = LDS_RING + (4 * g + (c >> 2)) * 352 + (3 * w) * 32 + (c & 3) * 8; wbB[0] = wbA[0] + (w == 3 ? 32 : 64);
    wbA[1] = wbA[0] + 4 * W1CH; wbB[1] = wbB[0] + 4 * W1CH;
    pin(wbA[0]); pin(wbA[1]); pin(wbB[0]); pin(wbB[1]);
    u32x4 xf[2][5], wf[2][3];
#define TRH(dst, h, addr) do { const u32x2 t_ = ldstr(L, addr); dst[2 * (h)] = t_[0]; dst[2 * (h) + 1] = t_[1]; } while (0)
    xf[0][4] = u32x4{0x3f803f80u, 0x3f803f80u, 0x3f803f80u, 0x3f803f80u}; pin(xf[0][4]); xf[1][4] = xf[0][4];
#define RD1(n, b, r) do { const int sl_ = ((n) + 5) % NSLOT, so_ = (sl_ & 3) * W1CH, wbase = wbA[sl_ >> 2], wbase2 = wbB[sl_ >> 2]; \
      if ((r) < 2) TRH(wf[b][0], (r) & 1, wbase + so_ + ((r) & 1) * 16 * 352); \
      else if ((r) < 6) xf[b][((r) - 2) & 3] = lds128(L, xo + (((r) - 2) & 3) * 16 * XROW); \
      else if ((r) < 8) TRH(wf[b][1], (r) & 1, wbase + so_ + 32 + ((r) & 1) * 16 * 352); \
      else TRH(wf[b][2], (r) & 1, wbase2 + so_ + ((r) & 1) * 16 * 352); } while (0)
    prog_wait(L, gP0, 1);
    prog_wait(L, gL, PL_W1 + 1);
    {
      const int xo = xbase + rot1 * 16;
#pragma unroll
      for (int r = 0; r < 10; ++r) RD1(0, 0, r);
    }
    prog_set(L, w, lane, PC_W1);
    bool bad = false;
#pragma unroll
    for (int n = 0; n < 16; ++n) {
      if (__builtin_expect(bad, 0)) prog_spin(L, gL, PL_W1 + n + 1);
      const int xo = xbase + ((n + 1 + rot1) & 15) * 16;
      u32 pv = 0;
      SCHED();
#pragma unroll
      for (int q = 0; q < 15; ++q) {
        const int u = q / 5, t = q % 5;
        MFA(n == 0, acc[u][t], wf[n & 1][u], xf[n & 1][t]);
        if (n + 1 < 16) {
          if (q < 10) RD1(n + 1, (n + 1) & 1, q);
          if (q == 10) prog_set(L, w, lane, PC_W1 + n + 1);
          if (q == 7 && n + 2 < 16) pv = prog_get(L, gL);
          if (q == 13 && n + 2 < 16) bad = prog_bad(pv, PL_W1 + n + 2);
          if (n == 0 && q == 1) st128(tv0, rws, toff);
          if (n == 0 && q == 3) st128(tv1, rws, toff + 16 * 8192);
          if (n == TFSTEP - 1 && q == 11) tag = *(LAS u32x2*)(L + LDS_TAG + lane * 8);
          if (n == TFSTEP && q == 3) { WAITV(0); st_flag(tag, rws, WS_FT + ((j * 4 + w) * 32 + i) * 128, lane); }
        }
        SCHED();
      }
      if (n + 2 >= 16) bad = false;
    }
    MFMA_SETTLE();
  }
  STAMP(5);
  {
    const int hlo = WS_H + (i * 4 + (((g & 1) + rotx) & 3)) * 43 * 1024 + ((g >> 1) * 16 + c) * 16;
    const int hhi = WS_H + (i * 4 + (((g & 1) + 2 + rotx) & 3)) * 43 * 1024 + ((g >> 1) * 16 + c) * 16;
    float mrow[4], rrow[4], r2row[4];
#pragma unroll
    for (int t = 0; t < 4; ++t) {
      mrow[t] = -*(LAS float*)(L + LDS_ST2 + w * 512 + (16 * t + c) * 4);
      rrow[t] = *(LAS float*)(L + LDS_ST2 + w * 512 + 256 + (16 * t + c) * 4);
      r2row[t] = rrow[t] * -1.4426950408889634f;
    }
#pragma unroll
    for (int u = 0; u < 3; ++u) {
      if (3 * w + u < nt) {
        u32 hp[4][2];
        {
          f32x2 tv[8], ar[8];
#pragma unroll
          for (int e = 0; e < 8; ++e) {
            const int t = e >> 1, hh = e & 1;
            tv[e] = f32x2{acc[u][4][2 * hh], acc[u][4][2 * hh + 1]} * mrow[t] + f32x2{acc[u][t][2 * hh], acc[u][t][2 * hh + 1]};
          }
          SCHED();
#pragma unroll
          for (int e = 0; e < 8; ++e) ar[e] = tv[e] * r2row[e >> 1];
          SCHED();
#pragma unroll
          for (int e = 0; e < 8; ++e) { ar[e] = f32x2{__builtin_amdgcn_exp2f(ar[e][0]), __builtin_amdgcn_exp2f(ar[e][1])}; tv[e] *= rrow[e >> 1]; }
          SCHED();
#pragma unroll
          for (int e = 0; e < 8; ++e) ar[e] += 1.f;
          SCHED();
#pragma unroll
          for (int e = 0; e < 8; ++e) ar[e] = f32x2{__builtin_amdgcn_rcpf(ar[e][0]), __builtin_amdgcn_rcpf(ar[e][1])};
          SCHED();
#pragma unroll
          for (int e = 0; e < 8; ++e) tv[e] *= ar[e];
          SCHED();
#pragma unroll
          for (int e = 0; e < 8; ++e) hp[e >> 1][e & 1] = pk_bf16v(tv[e]);
        }
#pragma unroll
        for (int tp = 0; tp < 2; ++tp) {
          auto a = __builtin_amdgcn_permlane16_swap(hp[2 * tp][0], hp[2 * tp + 1][0], false, false);
          auto b = __builtin_amdgcn_permlane16_swap(hp[2 * tp][1], hp[2 * tp + 1][1], false, false);
          const u32x4 o = {a[0], b[0], a[1], b[1]};
          st128(o, rws, (tp ? hhi : hlo) + (u0 + 3 * w + u) * 512);
        }
      }
    }
  }
  STAMPX(6);
  const int orow = 32 * (16 * w + c) + i3;
  {
    const float st = (float)(2 * (64 * j3 + 16 * w + c) + 1) * (1.f / 1024.f);
    const float K = 2.f * cosrev(32.f * st);
    int fa = LDS_FI + w * 1024 + lane * 16; pin(fa);
#pragma unroll
    for (int reim = 0; reim < 2; ++reim) {
      u32x4 fo[8];
#pragma unroll
      for (int r = 0; r < 4; ++r) {
        const float a0 = (float)(8 * g + 2 * r) * st + (reim ? 0.25f : 0.f), a1 = a0 + 32.f * st;
        f32x2 pa = {cosrev(a0), cosrev(a0 + st)}, pb = {cosrev(a1), cosrev(a1 + st)};
        f32x2 p0 = pa;
        if (reim == 1 && r == 0 && g == 0) p0[0] = (c & 1) ? -1.f : 1.f;
        fo[0][r] = pk_bf16v(p0); fo[1][r] = pk_bf16v(pb);
#pragma unroll
        for (int jk = 2; jk < 8; ++jk) { const f32x2 pc = K * pb - pa; pa = pb; pb = pc; fo[jk][r] = pk_bf16v(pc); }
      }
#pragma unroll
      for (int jk = 0; jk < 8; ++jk) pin(fo[jk]);
      if (reim == 0) prog_wait(L, gC, PC_W1 + 15);
#pragma unroll
      for (int jk = 0; jk < 8; ++jk) *(LAS u32x4*)(L + fa + ((reim * 8 + jk - rotA) & 15) * 4096) = fo[jk];
      if (reim == 0) { WAITV(0); st_flag(tag, rws, WS_FH + ((i * 8 + j) * 4 + w) * 128, lane); }
    }
  }
  prog_set(L, w, lane, PC_FI);
  STAMPX(7);
  u32x4 gi[2];
  {
    const float sf = (float)(16 * w + c) * (1.f / 64.f);
#pragma unroll
    for (int kp = 0; kp < 2; ++kp)
#pragma unroll
      for (int r = 0; r < 4; ++r) {
        const float a = (float)(16 * kp + 4 * g + r) * sf;
        float hi = cosrev(a - 0.25f);
        if (kp == 0 && r == 0 && g == 0) hi = (c & 1) ? -1.f : 1.f;
        gi[kp][r] = pk_bf16(cosrev(a), hi);
      }
    pin(gi[0]); pin(gi[1]);
  }
  u32x2 xv[4];
#pragma unroll
  for (int nu = 0; nu < 4; ++nu) xv[nu] = *(const GAS u32x2*)(x + orow * D_ + 64 * j3 + 16 * nu + 4 * g);
  f32x4 oa[4], ob[4];
  {
    const int kh = w & 1;
    constexpr int HW = 4;
    u32x4 ha[HW], hb[HW];
    const int hoa = WS_H + (i3 * 4 + w) * 43 * 1024 + lane * 16, hob = WS_H + (i3 * 4 + (w ^ 1)) * 43 * 1024 + lane * 16;
    u32 hso = (kh + rot2) * 1024; if (hso >= 43 * 1024) hso -= 43 * 1024;
#define HNEXT() do { hso += 2048; if (hso >= 43 * 1024) hso -= 43 * 1024; } while (0)
    int wb1[4], wb2[4];
    constexpr int WB1 = LDS_RING, WB2 = LDS_RING + 7 * 8192;
    {
      const int row = 8 * g + (c >> 2), swz = ((row >> 1) & 1) | (((row >> 3) & 1) << 1);
#pragma unroll
      for (int nu = 0; nu < 4; ++nu) {
        wb1[nu] = WB1 + kh * 4096 + row * 128 + ((nu ^ swz) * 32) + (c & 3) * 8; wb2[nu] = wb1[nu] + (WB2 - WB1);
        pin(wb1[nu]); pin(wb2[nu]);
      }
    }
#define W2ADDR(nu, un) (u2slot(un) >= WB2 ? wb2[nu] + (u2slot(un) - WB2) : wb1[nu] + (u2slot(un) - WB1))
    u32x4 wf[U2N][4];
#define W2RD(k) TRH(wf[((k) >> 3) % U2N][((k) & 7) >> 1], (k) & 1, W2ADDR(((k) & 7) >> 1, (k) >> 3) + ((k) & 1) * 512)
    prog_wait(L, gL, PL_W2 + U2N - 1);
    STAMPX(8);
#pragma unroll
    for (int k = 0; k < 8 * U2N; ++k) { W2RD(k); if ((k & 7) == 7) prog_set(L, w, lane, pcw2(k >> 3)); }
#pragma unroll
    for (int n = 0; n < U2N; ++n)
#pragma unroll
      for (int r = 0; r < 4; ++r) pin(wf[n][r]);
    STAMPX(9);
    if (w == 0) {
      flag_wait<1, 2>(rws, WS_FH + i3 * 32 * 128, lane, 32, tag);
      prog_set(L, w, lane, PC_HS);
    } else prog_wait(L, pga_of(0, 1, lane), PC_HS);
    STAMPX(10);
#pragma unroll
    for (int n = 0; n < HW; ++n) {
      ha[n] = __builtin_amdgcn_raw_buffer_load_b128(rws, hoa, hso, SC1); SCHED();
      hb[n] = __builtin_amdgcn_raw_buffer_load_b128(rws, hob, hso, SC1);
      HNEXT(); SCHED();
    }
    prog_wait(L, gL, PL_W2 + 2 * U2N - 1);
    bool bad = false;
#pragma unroll
    for (int un = 0; un < 21; ++un) {
      const int k0 = 8 * U2N + 6 * (un - 1), kn1 = 8 * U2N + 6 * (un + 1) - 1 < 175 ? 8 * U2N + 6 * (un + 1) - 1 : 175;
      const int need = (k0 + 5 < 175 ? k0 + 5 : 175) >> 3;
      if (__builtin_expect(bad, 0)) prog_spin(L, gL, PL_W2 + need);
      bad = false;
      const bool chk = (kn1 >> 3) >= 2 * U2N && k0 + 6 < 176;
      u32 pv = 0;
      SCHED();
#pragma unroll
      for (int q = 0; q < 8; ++q) {
        const int nu = q >> 1;
        const u32x4 wa = wf[un % U2N][nu];
        if (q & 1) MFA(un == 0, ob[nu], wa, hb[un % HW]); else MFA(un == 0, oa[nu], wa, ha[un % HW]);
        if (q == 0 && chk) pv = prog_get(L, gL);
        if (un >= 1 && q < 6 && k0 + q < 176) {
          W2RD(k0 + q);
          if (((k0 + q) & 7) == 7 && ((k0 + q) >> 3) + U2N < 22) prog_set(L, w, lane, pcw2((k0 + q) >> 3));
        }
        if (q == 5 && chk) bad = prog_bad(pv, PL_W2 + (kn1 >> 3));
        if (un + HW < 22) {
          if (q == 6) ha[un % HW] = __builtin_amdgcn_raw_buffer_load_b128(rws, hoa, hso, SC1);
          if (q == 7) { hb[un % HW] = __builtin_amdgcn_raw_buffer_load_b128(rws, hob, hso, SC1); HNEXT(); }
        }
        SCHED();
      }
    }
    if (kh == 0) {
#pragma unroll
      for (int nu = 0; nu < 4; ++nu) {
        const u32x4 wa = wf[21 % U2N][nu];
        mfa(oa[nu], wa, ha[21 % HW]); mfa(ob[nu], wa, hb[21 % HW]);
      }
    }
    MFMA_SETTLE();
  }
  STAMPX(11);
  prog_wait(L, gP, 6);
#pragma unroll
  for (int nu = 0; nu < 4; ++nu) *(LAS f32x4*)(L + LDS_RED + ((w ^ 1) * 4 + nu) * 1024 + lane * 16) = ob[nu];
  prog_set(L, w, lane, PC_RED);
  {
    u32x4 pf[8];
#pragma unroll
    for (int n = 0; n < 8; ++n) pf[n] = lds128(L, LDS_PM + (16 * (n >> 1) + c) * PMROW + (32 * (n & 1) + 8 * g) * 2);
#pragma unroll
    for (int nu = 0; nu < 4; ++nu) oa[nu] = mfma_bf(pf[2 * nu], gi[0], oa[nu]);
#pragma unroll
    for (int nu = 0; nu < 4; ++nu) oa[nu] = mfma_bf(pf[2 * nu + 1], gi[1], oa[nu]);
  }
  prog_wait(L, pga_of(w ^ 1, 1, lane), PC_RED);
#pragma unroll
  for (int nu = 0; nu < 4; ++nu) {
    const int col = 64 * j3 + 16 * nu + 4 * g;
    const f32x4 v = oa[nu] + *(LAS f32x4*)(L + LDS_RED + (w * 4 + nu) * 1024 + lane * 16);
    const u32x2 o = {pk_bf16(v[0] + bf2f(xv[nu][0] & 0xffffu), v[1] + asf(xv[nu][0] & 0xffff0000u)),
                     pk_bf16(v[2] + bf2f(xv[nu][1] & 0xffffu), v[3] + asf(xv[nu][1] & 0xffff0000u))};
    *(GAS u32x2*)(out + orow * D_ + col) = o;
  }
  STAMPX(12);
  STAMPX(13);
  STAMP_OUT();
}

#undef x
#undef wr
#undef wi
#undef w1
#undef w2
#undef ws
#undef out
extern "C" void kernel_launch(void* const* d_in, const int* in_sizes, int n_in, void* d_out, int out_size, void* d_ws, size_t ws_size,
                              hipStream_t stream) {
  Args a;
  a.x = (const u16*)d_in[0]; a.wr = (const u16*)d_in[1]; a.wi = (const u16*)d_in[2];
  a.w1 = (const u16*)d_in[4]; a.w2 = (const u16*)d_in[5]; a.ws = (unsigned char*)d_ws; a.out = (u16*)d_out;
  (void)hipFuncSetAttribute((const void*)k_all, hipFuncAttributeMaxDynamicSharedMemorySize, LDS_BYTES);
  k_all<<<256, 512, LDS_BYTES, stream>>>(a);
}
```
